```python
import torch

def _softmax(x, axis=-1):
    return torch.softmax(x, dim=axis)
def _gelu(x):
    return torch.nn.functional.gelu(x, approximate="tanh")
def _silu(x):
    return torch.nn.functional.silu(x)
def _sigmoid(x):
    return torch.sigmoid(x)
def _layernorm(x, axis=-1, eps=1e-5):
    m = x.mean(dim=axis, keepdim=True)
    v = x.var(dim=axis, keepdim=True, unbiased=False)
    return (x - m) / torch.sqrt(v + eps)
def _scatter_add(tgt, idx, src, axis=0):
    return tgt.clone().index_add_(axis, idx, src)
def _segment_sum(src, seg, n):
    out = torch.zeros((n,) + src.shape[1:], dtype=src.dtype)
    return out.index_add_(0, seg, src)
def _pad(x, pw):
    flat = tuple(v for pair in reversed(pw) for v in pair)
    return torch.nn.functional.pad(x, flat)

kernel_name = 'gated_mix__grouped_kv_mqa__sliding_block__prompt'

def setup_inputs():

    _w = (lambda a: (lambda t: t.to(torch.bfloat16) if t.is_floating_point() else t)(torch.as_tensor(a)))
    def _A_setup():
        B, S, D, H = 1, 2048, 1024, 32
        Dh = D // H
        return {
            "x": _w((lambda *s: torch.randn(*s, dtype=torch.bfloat16) * 0.05)(B, S, D)),
            "wq": _w((lambda *s: torch.randn(*s, dtype=torch.bfloat16) * 0.8)(D, H * Dh)),
            "wk": _w((lambda *s: torch.randn(*s, dtype=torch.bfloat16) * 0.8)(D, Dh)),
            "wv": _w((lambda *s: torch.randn(*s, dtype=torch.bfloat16) * 0.2)(D, Dh)),
            "wo": _w((lambda *s: torch.randn(*s, dtype=torch.bfloat16) * 0.2)(H * Dh, D)),
        }
    _A_in = _A_setup()

    def _B_setup():
        B, S, D = 1, 2048, 1024
        return {
            "x": _w((lambda *s: torch.randn(*s, dtype=torch.bfloat16) * 0.05)(B, S, D)),
            "wqkv": _w((lambda *s: torch.randn(*s, dtype=torch.bfloat16) * 0.8)(D, 3 * D)),
            "wo": _w((lambda *s: torch.randn(*s, dtype=torch.bfloat16) * 0.05)(D, D)),
        }
    _B_in = _B_setup()

    return {
        "A_x": _A_in["x"],
        "A_wq": _A_in["wq"],
        "A_wk": _A_in["wk"],
        "A_wv": _A_in["wv"],
        "A_wo": _A_in["wo"],
        "B_x": _B_in["x"],
        "B_wqkv": _B_in["wqkv"],
        "B_wo": _B_in["wo"],
        "gate_w": _w((lambda *s: torch.randn(*s, dtype=torch.bfloat16) * 0.05)(1024) * 16.0),
    }

def reference(A_x, A_wq, A_wk, A_wv, A_wo, B_x, B_wqkv, B_wo, gate_w):
    def _A_ref(x, wq, wk, wv, wo):
        B, S, D = x.shape
        H = 32
        Dh = D // H
        q = (lambda x, *d: x.permute(*d))((x @ wq).reshape(B, S, H, Dh), 0, 2, 1, 3)
        k = x @ wk
        v = x @ wv
        scores = torch.einsum("bhqd,bkd->bhqk", q, k) * (Dh ** -0.5)
        p = _softmax(scores, axis=-1)
        o = torch.einsum("bhqk,bkd->bhqd", p, v)
        return (lambda x, *d: x.permute(*d))(o, 0, 2, 1, 3).reshape(B, S, H * Dh) @ wo

    def _B_ref(x, wqkv, wo):
        B, S, D = x.shape
        H = 32; Dh = D // H; W = 256
        qkv = (lambda x, *d: x.permute(*d))((x @ wqkv).reshape(B, S, 3, H, Dh), 2, 0, 3, 1, 4)
        q, k, v = qkv[0], qkv[1], qkv[2]
        scores = torch.einsum("bhqd,bhkd->bhqk", q, k) * (Dh ** -0.5)
        idx = torch.arange(S)
        mask = (idx[:, None] - idx[None, :] >= 0) & (idx[:, None] - idx[None, :] < W)
        scores = torch.where(mask[None, None], scores, -1e9)
        p = _softmax(scores, axis=-1)
        o = torch.einsum("bhqk,bhkd->bhqd", p, v)
        return (lambda x, *d: x.permute(*d))(o, 0, 2, 1, 3).reshape(B, S, D) @ wo

    x = A_x
    g = _sigmoid((x * gate_w).sum(-1, keepdims=True))
    a = _A_ref(x, A_wq, A_wk, A_wv, A_wo)
    b = _B_ref(x, B_wqkv, B_wo)
    return g * a + (1 - g) * b
```

```cpp
#include <hip/hip_runtime.h>
#include <hip/hip_bf16.h>
#include <stdint.h>

#define DI __device__ __forceinline__
typedef __attribute__((ext_vector_type(8))) __bf16 bf16x8;
typedef __attribute__((ext_vector_type(2))) __bf16 bf16x2;
typedef __attribute__((ext_vector_type(4))) short s16x4;
typedef __attribute__((ext_vector_type(8))) short s16x8;
typedef __attribute__((ext_vector_type(2))) float f32x2;
typedef __attribute__((ext_vector_type(4))) float f32x4;
typedef __attribute__((ext_vector_type(16))) float f32x16;
typedef __attribute__((ext_vector_type(4))) unsigned u32x4;
typedef __attribute__((address_space(3))) const char* lds_cptr;
typedef __attribute__((address_space(3))) char* lds_ptr;
typedef __amdgpu_buffer_rsrc_t srd_t;

constexpr int SEQ = 2048, DM = 1024, NH = 32;
constexpr float QK_SCALE = 0.17677669529663687f * 1.4426950408889634f;

constexpr unsigned WS_QA = 0;
constexpr unsigned WS_QB = 4u << 20;
constexpr unsigned WS_KVA = 8u << 20;
constexpr unsigned WS_G = (8u << 20) + (256u << 10);
constexpr unsigned WS_KVB = 9u << 20;
constexpr unsigned WS_O = 17u << 20;
constexpr unsigned WS_END = 25u << 20;

#define MFMA32(a, b, c) __builtin_amdgcn_mfma_f32_32x32x16_bf16((a), (b), (c), 0, 0, 0)
#define MFMA16(a, b, c) __builtin_amdgcn_mfma_f32_16x16x32_bf16((a), (b), (c), 0, 0, 0)
#define SBAR() __builtin_amdgcn_sched_barrier(0)
#define SGB(mask, n) __builtin_amdgcn_sched_group_barrier((mask), (n), 0)
#define BAR() asm volatile("s_barrier" ::: "memory")
#define WAITV_(n) asm volatile("s_waitcnt vmcnt(" #n ")" ::: "memory")
#define WAITV(n) WAITV_(n)
#define WAITL_BAR() asm volatile("s_waitcnt lgkmcnt(0)\n\ts_barrier" ::: "memory")
#define PIN(x) asm volatile("" : "+v"(x))

DI unsigned rfl(unsigned v) { return __builtin_amdgcn_readfirstlane(v); }
DI srd_t make_srd(const void* p, unsigned bytes) { return __builtin_amdgcn_make_buffer_rsrc((void*)p, (short)0, (int)bytes, 0x00020000); }
DI unsigned pack2(float lo, float hi) { f32x2 v = {lo, hi}; return __builtin_bit_cast(unsigned, __builtin_convertvector(v, bf16x2)); }
DI u32x4 pack8(const f32x16& a, int s) {
    u32x4 r; r[0] = pack2(a[8 * s + 0], a[8 * s + 1]); r[1] = pack2(a[8 * s + 2], a[8 * s + 3]);
    r[2] = pack2(a[8 * s + 4], a[8 * s + 5]); r[3] = pack2(a[8 * s + 6], a[8 * s + 7]); return r; }
DI bf16x8 as_frag(const u32x4& v) { return __builtin_bit_cast(bf16x8, v); }
DI bf16x8 lds_frag(lds_cptr p) { return *(const __attribute__((address_space(3))) bf16x8*)p; }
DI s16x4 lds_tr(lds_cptr p) {
    typedef short v4i16_t __attribute__((ext_vector_type(4)));
    return __builtin_amdgcn_ds_read_tr16_b64_v4i16((__attribute__((address_space(3))) v4i16_t*)p); }
DI bf16x8 join_tr(const s16x4& lo, const s16x4& hi) { return __builtin_bit_cast(bf16x8, __builtin_shufflevector(lo, hi, 0, 1, 2, 3, 4, 5, 6, 7)); }
DI void dma16(srd_t srd, unsigned voff, unsigned soff, unsigned m0v) {
    unsigned keep;
    asm volatile("s_mov_b32 %0, m0\n\ts_mov_b32 m0, %4\n\ts_nop 0\n\tbuffer_load_dwordx4 %1, %2, %3 offen lds\n\ts_mov_b32 m0, %0"
                 : "=&s"(keep) : "v"(voff), "s"(srd), "s"(soff), "s"(m0v) : "memory"); }
DI void st16(srd_t srd, unsigned voff, const u32x4& v) { __builtin_amdgcn_raw_buffer_store_b128(v, srd, voff, 0, 0); }
DI u32x4 ld16(srd_t srd, unsigned voff) { return __builtin_amdgcn_raw_buffer_load_b128(srd, voff, 0, 0); }

constexpr int P_STAGE = 32768 + 16384 + 4096;
constexpr int P_GATE = 3 * P_STAGE;
constexpr int P_LDS = P_GATE + 2048;

template <bool SWAP>
DI void proj_consume(lds_cptr L, int wm, int wn, int lane, f32x16 (&acc)[4][2]) {
    const int r = lane & 31, h = lane >> 5, g1 = (lane >> 4) & 1, q = (lane & 15) >> 2, pp = lane & 3;
    unsigned xo[4], wo[2];
#pragma unroll
    for (int ks = 0; ks < 4; ++ks) xo[ks] = (unsigned)((wm * 128 + r) * 128 + (((2 * ks + h) ^ ((r >> 1) & 7)) << 4));
#pragma unroll
    for (int jn = 0; jn < 2; ++jn) wo[jn] = (unsigned)(32768 + (8 * h + q) * 256 + (((((wn * 2 + jn) ^ q) << 2) | (2 * g1 + (pp >> 1))) << 4) + 8 * (pp & 1));
    bf16x8 xf[2][4]; s16x4 wl[2][2], wh[2][2];
#define P_RD(b, sl, ks) do { _Pragma("unroll") for (int i_ = 0; i_ < 4; ++i_) xf[b][i_] = lds_frag(L + (sl) + xo[ks] + i_ * 4096); \
        _Pragma("unroll") for (int j_ = 0; j_ < 2; ++j_) { wl[b][j_] = lds_tr(L + (sl) + wo[j_] + (ks) * 4096); wh[b][j_] = lds_tr(L + (sl) + wo[j_] + (ks) * 4096 + 1024); } } while (0)
#define P_MMA(b) do { _Pragma("unroll") for (int j_ = 0; j_ < 2; ++j_) { const bf16x8 wf_ = join_tr(wl[b][j_], wh[b][j_]); \
        _Pragma("unroll") for (int i_ = 0; i_ < 4; ++i_) acc[i_][j_] = SWAP ? MFMA32(wf_, xf[b][i_], acc[i_][j_]) : MFMA32(xf[b][i_], wf_, acc[i_][j_]); } } while (0)
#define P_ORDER() do { _Pragma("unroll") for (int i_ = 0; i_ < 4; ++i_) { SGB(0x008, 1); SGB(0x100, 2); } SGB(0x008, 4); SBAR(); } while (0)
    unsigned slot = 0;
    BAR();
    P_RD(0, slot, 0); SBAR();
    for (int t = 0; t < 16; ++t) {
        P_RD(1, slot, 1); P_MMA(0); P_ORDER();
        P_RD(0, slot, 2); P_MMA(1); P_ORDER();
        P_RD(1, slot, 3); P_MMA(0); P_ORDER();
        if (t < 15) {
            WAITL_BAR();
            slot = (slot == 2 * P_STAGE) ? 0u : slot + P_STAGE;
            P_RD(0, slot, 0); P_MMA(1); P_ORDER();
        } else { P_MMA(1); SBAR(); }
    }
#undef P_RD
#undef P_MMA
#undef P_ORDER
}

__global__ __launch_bounds__(512, 2) void k_proj(const __hip_bfloat16* __restrict__ xA, const __hip_bfloat16* __restrict__ wq,
                                                 const __hip_bfloat16* __restrict__ wk, const __hip_bfloat16* __restrict__ wv,
                                                 const __hip_bfloat16* __restrict__ xB, const __hip_bfloat16* __restrict__ wqkv,
                                                 const __hip_bfloat16* __restrict__ gate_w, char* __restrict__ ws) {
    __shared__ __attribute__((aligned(1024))) char smem[P_LDS];
    const lds_cptr L = (lds_cptr)smem;
    const unsigned lds0 = (unsigned)(uintptr_t)(lds_ptr)smem;
    const int lane = threadIdx.x & 63, wid = rfl(threadIdx.x >> 6);
    const int xcd = blockIdx.x & 7, li = blockIdx.x >> 3;
    const bool isA = xcd < 2;
    const int tb = isA ? xcd * 4 + (li & 3) : ((xcd - 2) & 1) * 4 + (li & 3);
    const int cb = isA ? (li >> 2) : ((xcd - 2) >> 1) * 8 + (li >> 2);
    const srd_t wsr = make_srd(ws, WS_END);
    const int r = lane & 31, h = lane >> 5;

    if (wid < 4) {
        const int wm = wid >> 1, wn = wid & 1;
        f32x16 acc[4][2];
#pragma unroll
        for (int i = 0; i < 4; ++i) { acc[i][0] = f32x16{}; acc[i][1] = f32x16{}; }
        const int kind = isA ? 0 : (cb >> 3);
        if (kind == 2) proj_consume<false>(L, wm, wn, lane, acc); else proj_consume<true>(L, wm, wn, lane, acc);
        const int head0 = (cb & 7) * 4 + wn * 2, tile0 = tb * 8 + wm * 4;
        const float sc = (kind == 1) ? QK_SCALE : 1.0f;
#pragma unroll
        for (int j = 0; j < 2; ++j)
#pragma unroll
            for (int i = 0; i < 4; ++i) {
                const unsigned ht = (unsigned)((head0 + j) * 64 + tile0 + i);
                const unsigned base = (kind == 0) ? ((isA ? WS_QA : WS_QB) + ht * 2048u) : (WS_KVB + ht * 4096u + (kind == 2 ? 2048u : 0u));
                f32x16 a = acc[i][j];
                if (kind == 1) {
#pragma unroll
                    for (int e = 0; e < 16; ++e) a[e] *= sc; }
                st16(wsr, base + lane * 16, pack8(a, 0));
                st16(wsr, base + 1024 + lane * 16, pack8(a, 1));
            }
        if (isA) { BAR(); BAR(); }
    } else {
        const int p = wid - 4;
        const unsigned ncols = isA ? 1024u : 3072u;
        const srd_t xs = make_srd(isA ? xA : xB, SEQ * DM * 2);
        const srd_t wsd = make_srd(isA ? wq : wqkv, DM * ncols * 2);
        const srd_t es = make_srd((cb & 1) ? wv : wk, DM * 32 * 2);
        unsigned xv[8], wvo[4];
#pragma unroll
        for (int i = 0; i < 8; ++i) { const int row = p * 64 + i * 8 + (lane >> 3); xv[i] = (unsigned)((tb * 256 + row) * 2048 + (((lane & 7) ^ ((row >> 1) & 7)) << 4)); }
#pragma unroll
        for (int i = 0; i < 4; ++i) { const int kr = p * 16 + i * 4 + (lane >> 4); wvo[i] = (unsigned)kr * ncols * 2u + (unsigned)(cb * 256) + (unsigned)(((lane & 15) ^ ((kr & 3) << 2)) << 4); }
        const unsigned ev = (unsigned)(p * 1024 + lane * 16);
        const unsigned wstep = 64u * ncols * 2u;
#define P_ISSUE(t, sl) do { const unsigned d_ = lds0 + (sl); \
        _Pragma("unroll") for (int i_ = 0; i_ < 8; ++i_) dma16(xs, xv[i_], (unsigned)(t) * 128u, d_ + p * 8192 + i_ * 1024); \
        _Pragma("unroll") for (int i_ = 0; i_ < 4; ++i_) dma16(wsd, wvo[i_], (unsigned)(t) * wstep, d_ + 32768 + p * 4096 + i_ * 1024); \
        if (isA) dma16(es, ev, (unsigned)(t) * 4096u, d_ + 49152 + p * 1024); } while (0)
        if (!isA) {
            P_ISSUE(0, 0); P_ISSUE(1, P_STAGE);
            WAITV(12); BAR();
            unsigned s2 = 2 * P_STAGE;
            for (int t = 0; t < 15; ++t) {
                if (t < 14) { P_ISSUE(t + 2, s2); s2 = (s2 == 2 * P_STAGE) ? 0u : s2 + P_STAGE; WAITV(12); } else { WAITV(0); }
                BAR();
            }
        } else {
            const srd_t gs = make_srd(gate_w, DM * 2);
            if (p < 2) dma16(gs, (unsigned)(p * 1024 + lane * 16), 0u, lds0 + P_GATE + p * 1024);
            P_ISSUE(0, 0); P_ISSUE(1, P_STAGE);
            WAITV(13); BAR();
            const int g1 = (lane >> 4) & 1, q = (lane & 15) >> 2, pp = lane & 3;
            const int xrow = (cb >> 1) * 64 + (p & 1) * 32 + r, ks0 = 2 * (p >> 1);
            unsigned xo[2];
#pragma unroll
            for (int u = 0; u < 2; ++u) xo[u] = (unsigned)(xrow * 128 + (((2 * (ks0 + u) + h) ^ ((r >> 1) & 7)) << 4));
            const unsigned eo = (unsigned)(49152 + (ks0 * 16 + 8 * h + q) * 64 + 32 * g1 + 8 * pp);
            const unsigned go = (unsigned)(P_GATE + (ks0 * 16 + 8 * h) * 2);
            f32x16 ea = f32x16{}; float ga = 0.f;
            const bool isK = !(cb & 1);
            unsigned s0 = 0, s2 = 2 * P_STAGE;
            for (int t = 0; t < 16; ++t) {
                if (t < 14) { P_ISSUE(t + 2, s2); s2 = (s2 == 2 * P_STAGE) ? 0u : s2 + P_STAGE; }
#pragma unroll
                for (int u = 0; u < 2; ++u) {
                    const bf16x8 xf = lds_frag(L + s0 + xo[u]);
                    const bf16x8 ef = join_tr(lds_tr(L + s0 + eo + u * 1024), lds_tr(L + s0 + eo + u * 1024 + 256));
                    if (isK) {
                        ea = MFMA32(ef, xf, ea);
                        const u32x4 gw = __builtin_bit_cast(u32x4, lds_frag(L + go + t * 128 + u * 32)), xw = __builtin_bit_cast(u32x4, xf);
#pragma unroll
                        for (int e = 0; e < 4; ++e) { const unsigned xe = xw[e], ge = gw[e];
                            ga = __builtin_amdgcn_fdot2_f32_bf16(__builtin_bit_cast(bf16x2, xe), __builtin_bit_cast(bf16x2, ge), ga, false); }
                    } else ea = MFMA32(xf, ef, ea);
                }
                s0 = (s0 == 2 * P_STAGE) ? 0u : s0 + P_STAGE;
                if (t < 15) {
                    if (t < 14) asm volatile("s_waitcnt vmcnt(13) lgkmcnt(0)\n\ts_barrier" ::: "memory");
                    else asm volatile("s_waitcnt vmcnt(0) lgkmcnt(0)\n\ts_barrier" ::: "memory");
                }
            }
            WAITL_BAR();
            float* red = (float*)smem + (p & 1) * 17 * 64;
            if (p >= 2) {
#pragma unroll
                for (int e = 0; e < 16; ++e) red[e * 64 + lane] = ea[e];
                red[16 * 64 + lane] = ga;
            }
            WAITL_BAR();
            if (p < 2) {
#pragma unroll
                for (int e = 0; e < 16; ++e) ea[e] += red[e * 64 + lane];
                ga += red[16 * 64 + lane];
                const unsigned tile = (unsigned)(tb * 8 + (cb >> 1) * 2 + p);
                if (isK) {
#pragma unroll
                    for (int e = 0; e < 16; ++e) ea[e] *= QK_SCALE;
                    ga += __shfl_xor(ga, 32);
                    const float g = 1.0f / (1.0f + __expf(-ga));
                    if (h == 0) __builtin_amdgcn_raw_buffer_store_b32(__float_as_uint(g), wsr, WS_G + (tile * 32 + r) * 4, 0, 0);
                }
                const unsigned base = WS_KVA + tile * 4096u + (isK ? 0u : 2048u);
                st16(wsr, base + lane * 16, pack8(ea, 0));
                st16(wsr, base + 1024 + lane * 16, pack8(ea, 1));
            }
        }
#undef P_ISSUE
    }
}

constexpr int A_SLOT = 32768, A_LDS = 3 * A_SLOT;
constexpr float NEG_INF = -__builtin_inff();

DI float half_max(const f32x16& s) {
    float a = fmaxf(fmaxf(s[0], s[1]), s[2]), b = fmaxf(fmaxf(s[3], s[4]), s[5]);
    a = fmaxf(fmaxf(a, s[6]), s[7]); b = fmaxf(fmaxf(b, s[8]), s[9]);
    a = fmaxf(fmaxf(a, s[10]), s[11]); b = fmaxf(fmaxf(b, s[12]), s[13]);
    a = fmaxf(fmaxf(a, s[14]), s[15]); a = fmaxf(a, b);
    auto rr = __builtin_amdgcn_permlane32_swap(__float_as_uint(a), __float_as_uint(a), false, false);
    return fmaxf(__uint_as_float(rr[0]), __uint_as_float(rr[1])); }
DI int crow(int e, int h) { return (e & 3) + 8 * (e >> 2) + 4 * h; }
DI f32x16 cblock(float base, int mode, int r, int h) {
    f32x16 c; const int hi = (mode == 1 ? r : 63) - 4 * h, lo = (mode == 2 ? r : -1) - 4 * h;
#pragma unroll
    for (int e = 0; e < 16; ++e) { const int k = crow(e, 0); c[e] = (k <= hi && k > lo) ? base : NEG_INF; }
    return c; }

DI void attn_safe(srd_t wsr, unsigned kvbase, int j0, int j1, int jdiag, int jfar, const bf16x8& q0, const bf16x8& q1, const bf16x8& ones, int lane, f32x16& O, f32x4& Lsum) {
    const int r = lane & 31, h = lane >> 5;
    float m = NEG_INF;
    for (int j = j0; j <= j1; ++j) {
        const unsigned a = kvbase + (unsigned)j * 4096u + lane * 16;
        const bf16x8 k0 = as_frag(ld16(wsr, a)), k1 = as_frag(ld16(wsr, a + 1024));
        f32x16 s = cblock(0.f, j == jdiag ? 1 : (j == jfar ? 2 : 0), r, h);
        s = MFMA32(k0, q0, s); s = MFMA32(k1, q1, s);
        m = fmaxf(m, half_max(s));
    }
    O = f32x16{}; Lsum = f32x4{};
    for (int j = j0; j <= j1; ++j) {
        const unsigned a = kvbase + (unsigned)j * 4096u + lane * 16;
        const bf16x8 k0 = as_frag(ld16(wsr, a)), k1 = as_frag(ld16(wsr, a + 1024)), v0 = as_frag(ld16(wsr, a + 2048)), v1 = as_frag(ld16(wsr, a + 3072));
        f32x16 s = cblock(-m, j == jdiag ? 1 : (j == jfar ? 2 : 0), r, h);
        s = MFMA32(k0, q0, s); s = MFMA32(k1, q1, s);
#pragma unroll
        for (int e = 0; e < 16; ++e) s[e] = __builtin_amdgcn_exp2f(s[e]);
        const bf16x8 p0 = as_frag(pack8(s, 0)), p1 = as_frag(pack8(s, 1));
        O = MFMA32(v0, p0, O); O = MFMA32(v1, p1, O);
        Lsum = MFMA16(ones, p0, Lsum); Lsum = MFMA16(ones, p1, Lsum);
    }
}
DI bool attn_bad(const f32x16& O, const f32x4& Lsum) {
    float a = fabsf(Lsum[0]);
#pragma unroll
    for (int e = 0; e < 16; ++e) a = fmaxf(a, fabsf(O[e]));
    bool bad = !(a < 1.0e37f);
#pragma unroll
    for (int e = 0; e < 16; ++e) bad |= (O[e] != O[e]);
    bad |= (Lsum[0] != Lsum[0]);
    return __any(bad) != 0; }

#define EX(S, i) S[i] = __builtin_amdgcn_exp2f(S[i])
#define CV(P, S, i) P[(i) & 3] = pack2(S[2 * (i)], S[2 * (i) + 1])
#define CYCLE(SC, SN, CN, ka, va, vb) do { \
    O = MFMA32(vf1, as_frag(p1), O); vf1 = lds_frag((va) + 3072); EX(SC, 2); EX(SC, 3); EX(SC, 4); PIN(SC); SBAR(); \
    Ls = MFMA16(ones, as_frag(p1), Ls); EX(SC, 5); PIN(SC); SBAR(); \
    SN = MFMA32(kf0, q0, CN); kf0 = lds_frag(ka); EX(SC, 6); EX(SC, 7); CV(p0, SC, 0); CV(p0, SC, 1); PIN(SC); PIN(p0); SBAR(); \
    SN = MFMA32(kf1, q1, SN); kf1 = lds_frag((ka) + 1024); CV(p0, SC, 2); CV(p0, SC, 3); EX(SC, 8); EX(SC, 9); PIN(SC); PIN(p0); SBAR(); \
    O = MFMA32(vf0, as_frag(p0), O); vf0 = lds_frag((vb) + 2048); EX(SC, 10); EX(SC, 11); EX(SC, 12); PIN(SC); SBAR(); \
    Ls = MFMA16(ones, as_frag(p0), Ls); EX(SC, 13); EX(SC, 14); EX(SC, 15); CV(p1, SC, 4); CV(p1, SC, 5); CV(p1, SC, 6); CV(p1, SC, 7); EX(SN, 0); EX(SN, 1); PIN(SC); PIN(SN); PIN(p1); SBAR(); \
    } while (0)

__global__ __launch_bounds__(512, 2) void k_attn(char* __restrict__ ws) {
    __shared__ __attribute__((aligned(1024))) char smem[A_LDS];
    const lds_cptr L = (lds_cptr)smem;
    const unsigned lds0 = (unsigned)(uintptr_t)(lds_ptr)smem;
    const int lane = threadIdx.x & 63, w = rfl(threadIdx.x >> 6);
    const int xcd = blockIdx.x & 7, li = blockIdx.x >> 3;
    const int head = xcd * 4 + (li & 3), tg = li >> 2, tile = tg * 8 + w;
    const srd_t wsr = make_srd(ws, WS_END);
    const int r = lane & 31, h = lane >> 5;
    const unsigned lv = (unsigned)lane * 16u;

    const int bt = tg * 8 - 8 + 2 * w;
    const unsigned kvb = WS_KVB + (unsigned)(head * 64 + (bt < 0 ? bt + 8 : bt)) * 4096u;
#pragma unroll
    for (int i = 0; i < 8; ++i) dma16(wsr, lv, kvb + (unsigned)i * 1024u, lds0 + (w * 8 + i) * 1024);
#pragma unroll
    for (int i = 0; i < 4; ++i) dma16(wsr, lv, WS_KVA + (unsigned)(w * 4 + i) * 1024u, lds0 + 2 * A_SLOT + (w * 4 + i) * 1024);
    const unsigned qoff = (unsigned)(head * 64 + tile) * 2048u + lv;
    bf16x8 q0 = as_frag(ld16(wsr, WS_QB + qoff)), q1 = as_frag(ld16(wsr, WS_QB + qoff + 1024));
    const bf16x8 qa0 = as_frag(ld16(wsr, WS_QA + qoff)), qa1 = as_frag(ld16(wsr, WS_QA + qoff + 1024));
    const float gate = __uint_as_float(__builtin_amdgcn_raw_buffer_load_b32(wsr, WS_G + (tile * 32 + r) * 4, 0, 0));
    const unsigned one2 = (((lane & 3) == 0) && (((lane >> 4) & 1) == ((lane >> 2) & 1))) ? 0x3F803F80u : 0u;
    const bf16x8 ones = as_frag((u32x4){one2, one2, one2, one2});

    f32x16 O, S0, S1; f32x4 Ls; bf16x8 kf0, kf1, vf0, vf1; u32x4 p0, p1;
    const unsigned obase = WS_O + (unsigned)tile * 131072u + (unsigned)head * 2048u + lv;

    WAITV(0); BAR();
    {
        const int nt = (tg == 0) ? w + 1 : 9;
        const lds_cptr kv = L + lv + (unsigned)(w + 8) * 4096u;
        kf0 = lds_frag(kv); kf1 = lds_frag(kv + 1024);
        S0 = cblock(0.f, 1, r, h);
        S0 = MFMA32(kf0, q0, S0); S0 = MFMA32(kf1, q1, S0);
        const float m = half_max(S0);
        f32x16 negm = cblock(-m, 0, r, h);
        f32x16 cfar = cblock(nt == 9 ? -m : NEG_INF, 2, r, h);
#pragma unroll
        for (int e = 0; e < 16; ++e) S0[e] -= m;
        kf0 = lds_frag(kv - 4096); kf1 = lds_frag(kv - 4096 + 1024);
        vf0 = lds_frag(kv + 2048); vf1 = as_frag((u32x4){0, 0, 0, 0}); p1 = (u32x4){0, 0, 0, 0};
        O = f32x16{}; Ls = f32x4{};
        EX(S0, 0); EX(S0, 1); SBAR();
#define BNEG(i) if (nt == (i)) negm = cblock(NEG_INF, 0, r, h)
        BNEG(1); CYCLE(S0, S1, negm, kv - 2 * 4096, kv, kv - 1 * 4096);
        BNEG(2); CYCLE(S1, S0, negm, kv - 3 * 4096, kv - 1 * 4096, kv - 2 * 4096);
        BNEG(3); CYCLE(S0, S1, negm, kv - 4 * 4096, kv - 2 * 4096, kv - 3 * 4096);
        BNEG(4); CYCLE(S1, S0, negm, kv - 5 * 4096, kv - 3 * 4096, kv - 4 * 4096);
        BNEG(5); CYCLE(S0, S1, negm, kv - 6 * 4096, kv - 4 * 4096, kv - 5 * 4096);
        BNEG(6); CYCLE(S1, S0, negm, kv - 7 * 4096, kv - 5 * 4096, kv - 6 * 4096);
        BNEG(7); CYCLE(S0, S1, negm, kv - 8 * 4096, kv - 6 * 4096, kv - 7 * 4096);
        CYCLE(S1, S0, cfar, kv - 8 * 4096, kv - 7 * 4096, kv - 8 * 4096);
        CYCLE(S0, S1, negm, kv - 8 * 4096, kv - 8 * 4096, kv - 8 * 4096);
#undef BNEG
        O = MFMA32(vf1, as_frag(p1), O); Ls = MFMA16(ones, as_frag(p1), Ls);
        if (attn_bad(O, Ls)) {
            const int jd = head * 64 + tile, jf = (nt == 9) ? jd - 8 : -1;
            attn_safe(wsr, WS_KVB, jd - (nt - 1), jd, jd, jf, q0, q1, ones, lane, O, Ls);
        }
        const float f = (1.0f - gate) / Ls[0];
#pragma unroll
        for (int e = 0; e < 16; ++e) O[e] *= f;
        st16(wsr, obase + 65536, pack8(O, 0)); st16(wsr, obase + 65536 + 1024, pack8(O, 1));
    }
    {
        q0 = qa0; q1 = qa1;
        asm volatile("s_waitcnt lgkmcnt(0)\n\ts_barrier" ::: "memory");
#define A_ISSUE(c, sl) do { _Pragma("unroll") for (int i_ = 0; i_ < 4; ++i_) \
            dma16(wsr, lv, WS_KVA + (unsigned)(c) * 32768u + (unsigned)(w * 4 + i_) * 1024u, lds0 + (sl) + (w * 4 + i_) * 1024); } while (0)
        A_ISSUE(1, 0);
        unsigned sc = 2 * A_SLOT, sn = 0, sf = A_SLOT;
        lds_cptr kv = L + lv + sc;
        kf0 = lds_frag(kv); kf1 = lds_frag(kv + 1024);
        S0 = MFMA32(kf0, q0, f32x16{}); S0 = MFMA32(kf1, q1, S0);
        const float m = half_max(S0);
        f32x16 negm = cblock(-m, 0, r, h);
#pragma unroll
        for (int e = 0; e < 16; ++e) S0[e] -= m;
        kf0 = lds_frag(kv + 4096); kf1 = lds_frag(kv + 4096 + 1024);
        vf0 = lds_frag(kv + 2048); vf1 = as_frag((u32x4){0, 0, 0, 0}); p1 = (u32x4){0, 0, 0, 0};
        O = f32x16{}; Ls = f32x4{};
        EX(S0, 0); EX(S0, 1); SBAR();
        for (int c = 0; c < 8; ++c) {
            const lds_cptr kn = L + lv + sn;
            CYCLE(S0, S1, negm, kv + 2 * 4096, kv + 0 * 4096, kv + 1 * 4096);
            CYCLE(S1, S0, negm, kv + 3 * 4096, kv + 1 * 4096, kv + 2 * 4096);
            CYCLE(S0, S1, negm, kv + 4 * 4096, kv + 2 * 4096, kv + 3 * 4096);
            CYCLE(S1, S0, negm, kv + 5 * 4096, kv + 3 * 4096, kv + 4 * 4096);
            CYCLE(S0, S1, negm, kv + 6 * 4096, kv + 4 * 4096, kv + 5 * 4096);
            CYCLE(S1, S0, negm, kv + 7 * 4096, kv + 5 * 4096, kv + 6 * 4096);
            WAITV(0); BAR();
            if (c < 6) A_ISSUE(c + 2, sf);
            CYCLE(S0, S1, negm, kn, kv + 6 * 4096, kv + 7 * 4096);
            CYCLE(S1, S0, negm, kn + 4096, kv + 7 * 4096, kn);
            kv = kn; const unsigned t_ = sc; sc = sn; sn = sf; sf = t_;
        }
#undef A_ISSUE
        O = MFMA32(vf1, as_frag(p1), O); Ls = MFMA16(ones, as_frag(p1), Ls);
        if (attn_bad(O, Ls)) attn_safe(wsr, WS_KVA, 0, 63, -1, -1, q0, q1, ones, lane, O, Ls);
        const float f = gate / Ls[0];
#pragma unroll
        for (int e = 0; e < 16; ++e) O[e] *= f;
        st16(wsr, obase, pack8(O, 0)); st16(wsr, obase + 1024, pack8(O, 1));
    }
}

constexpr int O_STAGE = 32768 + 16384, O_LDS = 3 * O_STAGE;

__global__ __launch_bounds__(512, 2) void k_out(const __hip_bfloat16* __restrict__ woA, const __hip_bfloat16* __restrict__ woB,
                                                char* __restrict__ ws, __hip_bfloat16* __restrict__ out) {
    __shared__ __attribute__((aligned(1024))) char smem[O_LDS];
    const lds_cptr L = (lds_cptr)smem;
    const unsigned lds0 = (unsigned)(uintptr_t)(lds_ptr)smem;
    const int lane = threadIdx.x & 63, wid = rfl(threadIdx.x >> 6);
    const int xcd = blockIdx.x & 7, li = blockIdx.x >> 3;
    const int tb = (xcd >> 1) * 4 + (li & 3), nb = (xcd & 1) * 8 + (li >> 2);
    if (wid < 4) {
        const int wm = wid >> 1, wn = wid & 1;
        const int r = lane & 31, h = lane >> 5, g1 = (lane >> 4) & 1, q = (lane & 15) >> 2, pp = lane & 3;
        const unsigned oo = (unsigned)(wm * 2 * 8192 + lane * 16);
        const unsigned wo = (unsigned)(32768 + (4 * h + q) * 128 + (((wn * 4 + 2 * g1 + (pp >> 1)) ^ ((q >> 1) << 2)) << 4) + 8 * (pp & 1));
        f32x16 acc0 = f32x16{}, acc1 = f32x16{};
        bf16x8 of[2][4]; s16x4 wl[2][2], wh[2][2];
#define O_RD(b, sl, kk) do { _Pragma("unroll") for (int u_ = 0; u_ < 2; ++u_) { \
            of[b][2 * u_] = lds_frag(L + (sl) + oo + ((kk) + u_) * 1024); of[b][2 * u_ + 1] = lds_frag(L + (sl) + oo + 8192 + ((kk) + u_) * 1024); \
            wl[b][u_] = lds_tr(L + (sl) + wo + ((kk) + u_) * 2048); wh[b][u_] = lds_tr(L + (sl) + wo + ((kk) + u_) * 2048 + 1024); } } while (0)
#define O_MMA(b) do { _Pragma("unroll") for (int u_ = 0; u_ < 2; ++u_) { const bf16x8 wf_ = join_tr(wl[b][u_], wh[b][u_]); \
            acc0 = MFMA32(wf_, of[b][2 * u_], acc0); acc1 = MFMA32(wf_, of[b][2 * u_ + 1], acc1); } } while (0)
#define O_ORDER() do { _Pragma("unroll") for (int i_ = 0; i_ < 2; ++i_) { SGB(0x008, 1); SGB(0x100, 4); } SGB(0x008, 2); SBAR(); } while (0)
        unsigned slot = 0;
        BAR();
        O_RD(0, slot, 0); SBAR();
        for (int t = 0; t < 16; ++t) {
            O_RD(1, slot, 2); O_MMA(0); O_ORDER();
            O_RD(0, slot, 4); O_MMA(1); O_ORDER();
            O_RD(1, slot, 6); O_MMA(0); O_ORDER();
            if (t < 15) {
                WAITL_BAR();
                slot = (slot == 2 * O_STAGE) ? 0u : slot + O_STAGE;
                O_RD(0, slot, 0); O_MMA(1); O_ORDER();
            } else { O_MMA(1); SBAR(); }
        }
#undef O_RD
#undef O_MMA
#undef O_ORDER
        const srd_t os = make_srd(out, SEQ * DM * 2);
#pragma unroll
        for (int i = 0; i < 2; ++i) {
            const f32x16& a = i ? acc1 : acc0;
            const unsigned row = (unsigned)(tb * 128 + wm * 64 + i * 32 + r);
            const unsigned base = row * 2048u + (unsigned)(nb * 128 + wn * 64 + h * 16);
#pragma unroll
            for (int g = 0; g < 4; g += 2) {
                unsigned ax = pack2(a[4 * g], a[4 * g + 1]), ay = pack2(a[4 * g + 2], a[4 * g + 3]);
                unsigned bx = pack2(a[4 * g + 4], a[4 * g + 5]), by = pack2(a[4 * g + 6], a[4 * g + 7]);
                auto rx = __builtin_amdgcn_permlane32_swap(ax, bx, false, false);
                auto ry = __builtin_amdgcn_permlane32_swap(ay, by, false, false);
                st16(os, base + g * 16, (u32x4){rx[0], ry[0], rx[1], ry[1]});
            }
        }
    } else {
        const int p = wid - 4;
        const srd_t wsr = make_srd(ws, WS_END);
        const srd_t wa = make_srd(woA, DM * DM * 2), wb = make_srd(woB, DM * DM * 2);
        const unsigned ov = (unsigned)lane * 16u;
        unsigned wv[4];
#pragma unroll
        for (int i = 0; i < 4; ++i) { const int row = p * 32 + i * 8 + (lane >> 3); wv[i] = (unsigned)(row * 2048 + nb * 128 + (((lane & 7) ^ (((lane >> 4) & 1) << 2)) << 4)); }
        const unsigned obase = WS_O + (unsigned)(tb * 4 + p) * 131072u;
#define O_ISSUE(t, sl) do { const unsigned d_ = lds0 + (sl); \
        _Pragma("unroll") for (int i_ = 0; i_ < 8; ++i_) dma16(wsr, ov, obase + (unsigned)(t) * 8192u + i_ * 1024, d_ + p * 8192 + i_ * 1024); \
        if ((t) < 8) { _Pragma("unroll") for (int i_ = 0; i_ < 4; ++i_) dma16(wa, wv[i_], (unsigned)(t) * 262144u, d_ + 32768 + p * 4096 + i_ * 1024); } \
        else { _Pragma("unroll") for (int i_ = 0; i_ < 4; ++i_) dma16(wb, wv[i_], (unsigned)((t) - 8) * 262144u, d_ + 32768 + p * 4096 + i_ * 1024); } } while (0)
        O_ISSUE(0, 0); O_ISSUE(1, O_STAGE);
        WAITV(12); BAR();
        unsigned s2 = 2 * O_STAGE;
        for (int t = 0; t < 15; ++t) {
            if (t < 14) { O_ISSUE(t + 2, s2); s2 = (s2 == 2 * O_STAGE) ? 0u : s2 + O_STAGE; WAITV(12); } else { WAITV(0); }
            BAR();
        }
#undef O_ISSUE
    }
}

extern "C" void kernel_launch(void* const* d_in, const int* in_sizes, int n_in, void* d_out, int out_size, void* d_ws, size_t ws_size,
                              hipStream_t stream) {
    const __hip_bfloat16* xA = (const __hip_bfloat16*)d_in[0];
    const __hip_bfloat16* wq = (const __hip_bfloat16*)d_in[1];
    const __hip_bfloat16* wk = (const __hip_bfloat16*)d_in[2];
    const __hip_bfloat16* wv = (const __hip_bfloat16*)d_in[3];
    const __hip_bfloat16* woA = (const __hip_bfloat16*)d_in[4];
    const __hip_bfloat16* xB = xA;
    const __hip_bfloat16* wqkv = (const __hip_bfloat16*)d_in[6];
    const __hip_bfloat16* woB = (const __hip_bfloat16*)d_in[7];
    const __hip_bfloat16* gate_w = (const __hip_bfloat16*)d_in[8];
    char* ws = (char*)d_ws;
    k_proj<<<256, 512, 0, stream>>>(xA, wq, wk, wv, xB, wqkv, gate_w, ws);
    k_attn<<<256, 512, 0, stream>>>(ws);
    k_out<<<256, 512, 0, stream>>>(woA, woB, ws, (__hip_bfloat16*)d_out);
}
```

```cpp
#include <hip/hip_runtime.h>
#include <hip/hip_bf16.h>
#include <stdint.h>

#define DI __device__ __forceinline__
typedef __attribute__((ext_vector_type(8))) __bf16 bf16x8;
typedef __attribute__((ext_vector_type(2))) __bf16 bf16x2;
typedef __attribute__((ext_vector_type(4))) short s16x4;
typedef __attribute__((ext_vector_type(8))) short s16x8;
typedef __attribute__((ext_vector_type(2))) float f32x2;
typedef __attribute__((ext_vector_type(4))) float f32x4;
typedef __attribute__((ext_vector_type(16))) float f32x16;
typedef __attribute__((ext_vector_type(4))) unsigned u32x4;
typedef __attribute__((address_space(3))) const char* lds_cptr;
typedef __attribute__((address_space(3))) char* lds_ptr;
typedef __amdgpu_buffer_rsrc_t srd_t;

constexpr int SEQ = 2048, DM = 1024, NH = 32;
constexpr float QK_SCALE = 0.17677669529663687f * 1.4426950408889634f;

constexpr unsigned WS_QA = 0;
constexpr unsigned WS_QB = 4u << 20;
constexpr unsigned WS_KVA = 8u << 20;
constexpr unsigned WS_G = (8u << 20) + (256u << 10);
constexpr unsigned WS_KVB = 9u << 20;
constexpr unsigned WS_O = 17u << 20;
constexpr unsigned WS_END = 25u << 20;

#define MFMA32(a, b, c) __builtin_amdgcn_mfma_f32_32x32x16_bf16((a), (b), (c), 0, 0, 0)
#define MFMA16(a, b, c) __builtin_amdgcn_mfma_f32_16x16x32_bf16((a), (b), (c), 0, 0, 0)
#define SBAR() __builtin_amdgcn_sched_barrier(0)
#define SGB(mask, n) __builtin_amdgcn_sched_group_barrier((mask), (n), 0)
#define BAR() asm volatile("s_barrier" ::: "memory")
#define WAITV_(n) asm volatile("s_waitcnt vmcnt(" #n ")" ::: "memory")
#define WAITV(n) WAITV_(n)
#define WAITL_BAR() asm volatile("s_waitcnt lgkmcnt(0)\n\ts_barrier" ::: "memory")
#define PIN(x) asm volatile("" : "+v"(x))

#define STAMP(kern, idx) do { } while (0)
DI unsigned rfl(unsigned v) { return __builtin_amdgcn_readfirstlane(v); }
DI srd_t make_srd(const void* p, unsigned bytes) { return __builtin_amdgcn_make_buffer_rsrc((void*)p, (short)0, (int)bytes, 0x00020000); }
DI unsigned pack2(float lo, float hi) { f32x2 v = {lo, hi}; return __builtin_bit_cast(unsigned, __builtin_convertvector(v, bf16x2)); }
DI u32x4 pack8(const f32x16& a, int s) {
    u32x4 r; r[0] = pack2(a[8 * s + 0], a[8 * s + 1]); r[1] = pack2(a[8 * s + 2], a[8 * s + 3]);
    r[2] = pack2(a[8 * s + 4], a[8 * s + 5]); r[3] = pack2(a[8 * s + 6], a[8 * s + 7]); return r; }
DI bf16x8 as_frag(const u32x4& v) { return __builtin_bit_cast(bf16x8, v); }
DI bf16x8 lds_frag(lds_cptr p) { return *(const __attribute__((address_space(3))) bf16x8*)p; }
DI s16x4 lds_tr(lds_cptr p) {
    typedef short v4i16_t __attribute__((ext_vector_type(4)));
    return __builtin_amdgcn_ds_read_tr16_b64_v4i16((__attribute__((address_space(3))) v4i16_t*)p); }
DI bf16x8 join_tr(const s16x4& lo, const s16x4& hi) { return __builtin_bit_cast(bf16x8, __builtin_shufflevector(lo, hi, 0, 1, 2, 3, 4, 5, 6, 7)); }
DI void dma16(srd_t srd, unsigned voff, unsigned soff, unsigned m0v) {
    unsigned keep;
    asm volatile("s_mov_b32 %0, m0\n\ts_mov_b32 m0, %4\n\ts_nop 0\n\tbuffer_load_dwordx4 %1, %2, %3 offen lds\n\ts_mov_b32 m0, %0"
                 : "=&s"(keep) : "v"(voff), "s"(srd), "s"(soff), "s"(m0v) : "memory"); }
DI void st16(srd_t srd, unsigned voff, const u32x4& v) { __builtin_amdgcn_raw_buffer_store_b128(v, srd, voff, 0, 0); }
DI u32x4 ld16(srd_t srd, unsigned voff) { return __builtin_amdgcn_raw_buffer_load_b128(srd, voff, 0, 0); }

constexpr int P_STAGE = 32768 + 16384 + 4096;
constexpr int P_GATE = 3 * P_STAGE;
constexpr int P_LDS = P_GATE + 2048;

template <bool SWAP>
DI void proj_consume(lds_cptr L, int wm, int wn, int lane, f32x16 (&acc)[4][2], char* ws) {
    const int r = lane & 31, h = lane >> 5, g1 = (lane >> 4) & 1, q = (lane & 15) >> 2, pp = lane & 3;
    unsigned xo[4], wo[2];
#pragma unroll
    for (int ks = 0; ks < 4; ++ks) xo[ks] = (unsigned)((wm * 128 + r) * 128 + (((2 * ks + h) ^ ((r >> 1) & 7)) << 4));
#pragma unroll
    for (int jn = 0; jn < 2; ++jn) wo[jn] = (unsigned)(32768 + (8 * h + q) * 256 + (((((wn * 2 + jn) ^ q) << 2) | (2 * g1 + (pp >> 1))) << 4) + 8 * (pp & 1));
    bf16x8 xf[2][4]; s16x4 wl[2][2], wh[2][2];
#define P_RD(b, sl, ks) do { _Pragma("unroll") for (int i_ = 0; i_ < 4; ++i_) xf[b][i_] = lds_frag(L + (sl) + xo[ks] + i_ * 4096); \
        _Pragma("unroll") for (int j_ = 0; j_ < 2; ++j_) { wl[b][j_] = lds_tr(L + (sl) + wo[j_] + (ks) * 4096); wh[b][j_] = lds_tr(L + (sl) + wo[j_] + (ks) * 4096 + 1024); } } while (0)
#define P_MMA(b) do { _Pragma("unroll") for (int j_ = 0; j_ < 2; ++j_) { const bf16x8 wf_ = join_tr(wl[b][j_], wh[b][j_]); \
        _Pragma("unroll") for (int i_ = 0; i_ < 4; ++i_) acc[i_][j_] = SWAP ? MFMA32(wf_, xf[b][i_], acc[i_][j_]) : MFMA32(xf[b][i_], wf_, acc[i_][j_]); } } while (0)
#define P_ORDER() do { _Pragma("unroll") for (int i_ = 0; i_ < 4; ++i_) { SGB(0x008, 1); SGB(0x100, 2); } SGB(0x008, 4); SBAR(); } while (0)
    unsigned slot = 0;
    BAR();
    STAMP(0, 1);
    P_RD(0, slot, 0); SBAR();
    for (int t = 0; t < 16; ++t) {
        P_RD(1, slot, 1); P_MMA(0); P_ORDER();
        P_RD(0, slot, 2); P_MMA(1); P_ORDER();
        P_RD(1, slot, 3); P_MMA(0); P_ORDER();
        if (t < 15) {
            WAITL_BAR();
            slot = (slot == 2 * P_STAGE) ? 0u : slot + P_STAGE;
            P_RD(0, slot, 0); P_MMA(1); P_ORDER();
        } else { P_MMA(1); SBAR(); }
    }
#undef P_RD
#undef P_MMA
#undef P_ORDER
}

__global__ __launch_bounds__(512, 2) void k_proj(const __hip_bfloat16* __restrict__ xA, const __hip_bfloat16* __restrict__ wq,
                                                 const __hip_bfloat16* __restrict__ wk, const __hip_bfloat16* __restrict__ wv,
                                                 const __hip_bfloat16* __restrict__ xB, const __hip_bfloat16* __restrict__ wqkv,
                                                 const __hip_bfloat16* __restrict__ gate_w, char* __restrict__ ws) {
    __shared__ __attribute__((aligned(1024))) char smem[P_LDS];
    const lds_cptr L = (lds_cptr)smem;
    const unsigned lds0 = (unsigned)(uintptr_t)(lds_ptr)smem;
    const int lane = threadIdx.x & 63, wid = rfl(threadIdx.x >> 6);
    const int xcd = blockIdx.x & 7, li = blockIdx.x >> 3;
    const bool isA = xcd < 2;
    const int tb = isA ? xcd * 4 + (li & 3) : ((xcd - 2) & 1) * 4 + (li & 3);
    const int cb = isA ? (li >> 2) : ((xcd - 2) >> 1) * 8 + (li >> 2);
    const srd_t wsr = make_srd(ws, WS_END);
    const int r = lane & 31, h = lane >> 5;
    STAMP(0, 0);
    if (wid < 4) {
        const int wm = wid >> 1, wn = wid & 1;
        f32x16 acc[4][2];
#pragma unroll
        for (int i = 0; i < 4; ++i) { acc[i][0] = f32x16{}; acc[i][1] = f32x16{}; }
        const int kind = isA ? 0 : (cb >> 3);
        if (kind == 2) proj_consume<false>(L, wm, wn, lane, acc, ws); else proj_consume<true>(L, wm, wn, lane, acc, ws);
        STAMP(0, 2);
        const int head0 = (cb & 7) * 4 + wn * 2, tile0 = tb * 8 + wm * 4;
        const float sc = (kind == 1) ? QK_SCALE : 1.0f;
#pragma unroll
        for (int j = 0; j < 2; ++j)
#pragma unroll
            for (int i = 0; i < 4; ++i) {
                const unsigned ht = (unsigned)((head0 + j) * 64 + tile0 + i);
                const unsigned base = (kind == 0) ? ((isA ? WS_QA : WS_QB) + ht * 2048u) : (WS_KVB + ht * 4096u + (kind == 2 ? 2048u : 0u));
                f32x16 a = acc[i][j];
                if (kind == 1) {
#pragma unroll
                    for (int e = 0; e < 16; ++e) a[e] *= sc; }
                st16(wsr, base + lane * 16, pack8(a, 0));
                st16(wsr, base + 1024 + lane * 16, pack8(a, 1));
            }
        STAMP(0, 3);
        if (isA) { BAR(); BAR(); }
    } else {
        const int p = wid - 4;
        const unsigned ncols = isA ? 1024u : 3072u;
        const srd_t xs = make_srd(isA ? xA : xB, SEQ * DM * 2);
        const srd_t wsd = make_srd(isA ? wq : wqkv, DM * ncols * 2);
        const srd_t es = make_srd((cb & 1) ? wv : wk, DM * 32 * 2);
        unsigned xv[8], wvo[4];
#pragma unroll
        for (int i = 0; i < 8; ++i) { const int row = p * 64 + i * 8 + (lane >> 3); xv[i] = (unsigned)((tb * 256 + row) * 2048 + (((lane & 7) ^ ((row >> 1) & 7)) << 4)); }
#pragma unroll
        for (int i = 0; i < 4; ++i) { const int kr = p * 16 + i * 4 + (lane >> 4); wvo[i] = (unsigned)kr * ncols * 2u + (unsigned)(cb * 256) + (unsigned)(((lane & 15) ^ ((kr & 3) << 2)) << 4); }
        const unsigned ev = (unsigned)(p * 1024 + lane * 16);
        const unsigned wstep = 64u * ncols * 2u;
#define P_ISSUE(t, sl) do { const unsigned d_ = lds0 + (sl); \
        _Pragma("unroll") for (int i_ = 0; i_ < 8; ++i_) dma16(xs, xv[i_], (unsigned)(t) * 128u, d_ + p * 8192 + i_ * 1024); \
        _Pragma("unroll") for (int i_ = 0; i_ < 4; ++i_) dma16(wsd, wvo[i_], (unsigned)(t) * wstep, d_ + 32768 + p * 4096 + i_ * 1024); \
        if (isA) dma16(es, ev, (unsigned)(t) * 4096u, d_ + 49152 + p * 1024); } while (0)
        if (!isA) {
            P_ISSUE(0, 0); P_ISSUE(1, P_STAGE);
            WAITV(12); BAR();
            unsigned s2 = 2 * P_STAGE;
            for (int t = 0; t < 15; ++t) {
                if (t < 14) { P_ISSUE(t + 2, s2); s2 = (s2 == 2 * P_STAGE) ? 0u : s2 + P_STAGE; WAITV(12); } else { WAITV(0); }
                BAR();
            }
        } else {
            const srd_t gs = make_srd(gate_w, DM * 2);
            if (p < 2) dma16(gs, (unsigned)(p * 1024 + lane * 16), 0u, lds0 + P_GATE + p * 1024);
            P_ISSUE(0, 0); P_ISSUE(1, P_STAGE);
            WAITV(13); BAR();
            const int g1 = (lane >> 4) & 1, q = (lane & 15) >> 2, pp = lane & 3;
            const int xrow = (cb >> 1) * 64 + (p & 1) * 32 + r, ks0 = 2 * (p >> 1);
            unsigned xo[2];
#pragma unroll
            for (int u = 0; u < 2; ++u) xo[u] = (unsigned)(xrow * 128 + (((2 * (ks0 + u) + h) ^ ((r >> 1) & 7)) << 4));
            const unsigned eo = (unsigned)(49152 + (ks0 * 16 + 8 * h + q) * 64 + 32 * g1 + 8 * pp);
            const unsigned go = (unsigned)(P_GATE + (ks0 * 16 + 8 * h) * 2);
            f32x16 ea = f32x16{}; float ga = 0.f;
            const bool isK = !(cb & 1);
            unsigned s0 = 0, s2 = 2 * P_STAGE;
            for (int t = 0; t < 16; ++t) {
                if (t < 14) { P_ISSUE(t + 2, s2); s2 = (s2 == 2 * P_STAGE) ? 0u : s2 + P_STAGE; }
#pragma unroll
                for (int u = 0; u < 2; ++u) {
                    const bf16x8 xf = lds_frag(L + s0 + xo[u]);
                    const bf16x8 ef = join_tr(lds_tr(L + s0 + eo + u * 1024), lds_tr(L + s0 + eo + u * 1024 + 256));
                    if (isK) {
                        ea = MFMA32(ef, xf, ea);
                        const u32x4 gw = __builtin_bit_cast(u32x4, lds_frag(L + go + t * 128 + u * 32)), xw = __builtin_bit_cast(u32x4, xf);
#pragma unroll
                        for (int e = 0; e < 4; ++e) { const unsigned xe = xw[e], ge = gw[e];
                            ga = __builtin_amdgcn_fdot2_f32_bf16(__builtin_bit_cast(bf16x2, xe), __builtin_bit_cast(bf16x2, ge), ga, false); }
                    } else ea = MFMA32(xf, ef, ea);
                }
                s0 = (s0 == 2 * P_STAGE) ? 0u : s0 + P_STAGE;
                if (t < 15) {
                    if (t < 14) asm volatile("s_waitcnt vmcnt(13) lgkmcnt(0)\n\ts_barrier" ::: "memory");
                    else asm volatile("s_waitcnt vmcnt(0) lgkmcnt(0)\n\ts_barrier" ::: "memory");
                }
            }
            WAITL_BAR();
            float* red = (float*)smem + (p & 1) * 17 * 64;
            if (p >= 2) {
#pragma unroll
                for (int e = 0; e < 16; ++e) red[e * 64 + lane] = ea[e];
                red[16 * 64 + lane] = ga;
            }
            WAITL_BAR();
            if (p < 2) {
#pragma unroll
                for (int e = 0; e < 16; ++e) ea[e] += red[e * 64 + lane];
                ga += red[16 * 64 + lane];
                const unsigned tile = (unsigned)(tb * 8 + (cb >> 1) * 2 + p);
                if (isK) {
#pragma unroll
                    for (int e = 0; e < 16; ++e) ea[e] *= QK_SCALE;
                    ga += __shfl_xor(ga, 32);
                    const float g = 1.0f / (1.0f + __expf(-ga));
                    if (h == 0) __builtin_amdgcn_raw_buffer_store_b32(__float_as_uint(g), wsr, WS_G + (tile * 32 + r) * 4, 0, 0);
                }
                const unsigned base = WS_KVA + tile * 4096u + (isK ? 0u : 2048u);
                st16(wsr, base + lane * 16, pack8(ea, 0));
                st16(wsr, base + 1024 + lane * 16, pack8(ea, 1));
            }
        }
#undef P_ISSUE
    }
}

constexpr int A_SLOT = 32768, A_LDS = 3 * A_SLOT;
constexpr float NEG_INF = -__builtin_inff();

DI float half_max(const f32x16& s) {
    float a = fmaxf(fmaxf(s[0], s[1]), s[2]), b = fmaxf(fmaxf(s[3], s[4]), s[5]);
    a = fmaxf(fmaxf(a, s[6]), s[7]); b = fmaxf(fmaxf(b, s[8]), s[9]);
    a = fmaxf(fmaxf(a, s[10]), s[11]); b = fmaxf(fmaxf(b, s[12]), s[13]);
    a = fmaxf(fmaxf(a, s[14]), s[15]); a = fmaxf(a, b);
    auto rr = __builtin_amdgcn_permlane32_swap(__float_as_uint(a), __float_as_uint(a), false, false);
    return fmaxf(__uint_as_float(rr[0]), __uint_as_float(rr[1])); }
DI int crow(int e, int h) { return (e & 3) + 8 * (e >> 2) + 4 * h; }
DI f32x16 cblock(float base, int mode, int r, int h) {
    f32x16 c; const int hi = (mode == 1 ? r : 63) - 4 * h, lo = (mode == 2 ? r : -1) - 4 * h;
#pragma unroll
    for (int e = 0; e < 16; ++e) { const int k = crow(e, 0); c[e] = (k <= hi && k > lo) ? base : NEG_INF; }
    return c; }

DI void attn_safe(srd_t wsr, unsigned kvbase, int j0, int j1, int jdiag, int jfar, const bf16x8& q0, const bf16x8& q1, const bf16x8& ones, int lane, f32x16& O, f32x4& Lsum) {
    const int r = lane & 31, h = lane >> 5;
    float m = NEG_INF;
    for (int j = j0; j <= j1; ++j) {
        const unsigned a = kvbase + (unsigned)j * 4096u + lane * 16;
        const bf16x8 k0 = as_frag(ld16(wsr, a)), k1 = as_frag(ld16(wsr, a + 1024));
        f32x16 s = cblock(0.f, j == jdiag ? 1 : (j == jfar ? 2 : 0), r, h);
        s = MFMA32(k0, q0, s); s = MFMA32(k1, q1, s);
        m = fmaxf(m, half_max(s));
    }
    O = f32x16{}; Lsum = f32x4{};
    for (int j = j0; j <= j1; ++j) {
        const unsigned a = kvbase + (unsigned)j * 4096u + lane * 16;
        const bf16x8 k0 = as_frag(ld16(wsr, a)), k1 = as_frag(ld16(wsr, a + 1024)), v0 = as_frag(ld16(wsr, a + 2048)), v1 = as_frag(ld16(wsr, a + 3072));
        f32x16 s = cblock(-m, j == jdiag ? 1 : (j == jfar ? 2 : 0), r, h);
        s = MFMA32(k0, q0, s); s = MFMA32(k1, q1, s);
#pragma unroll
        for (int e = 0; e < 16; ++e) s[e] = __builtin_amdgcn_exp2f(s[e]);
        const bf16x8 p0 = as_frag(pack8(s, 0)), p1 = as_frag(pack8(s, 1));
        O = MFMA32(v0, p0, O); O = MFMA32(v1, p1, O);
        Lsum = MFMA16(ones, p0, Lsum); Lsum = MFMA16(ones, p1, Lsum);
    }
}
DI bool attn_bad(const f32x16& O, const f32x4& Lsum) {
    float a = fabsf(Lsum[0]);
#pragma unroll
    for (int e = 0; e < 16; ++e) a = fmaxf(a, fabsf(O[e]));
    bool bad = !(a < 1.0e37f);
#pragma unroll
    for (int e = 0; e < 16; ++e) bad |= (O[e] != O[e]);
    bad |= (Lsum[0] != Lsum[0]);
    return __any(bad) != 0; }

#define EX(S, i) S[i] = __builtin_amdgcn_exp2f(S[i])
#define CV(P, S, i) P[(i) & 3] = pack2(S[2 * (i)], S[2 * (i) + 1])
#define CYCLE(SC, SN, CN, ka, va, vb) do { \
    O = MFMA32(vf1, as_frag(p1), O); vf1 = lds_frag((va) + 3072); EX(SC, 2); EX(SC, 3); EX(SC, 4); PIN(SC); SBAR(); \
    Ls = MFMA16(ones, as_frag(p1), Ls); EX(SC, 5); PIN(SC); SBAR(); \
    SN = MFMA32(kf0, q0, CN); kf0 = lds_frag(ka); EX(SC, 6); EX(SC, 7); CV(p0, SC, 0); CV(p0, SC, 1); PIN(SC); PIN(p0); SBAR(); \
    SN = MFMA32(kf1, q1, SN); kf1 = lds_frag((ka) + 1024); CV(p0, SC, 2); CV(p0, SC, 3); EX(SC, 8); EX(SC, 9); PIN(SC); PIN(p0); SBAR(); \
    O = MFMA32(vf0, as_frag(p0), O); vf0 = lds_frag((vb) + 2048); EX(SC, 10); EX(SC, 11); EX(SC, 12); PIN(SC); SBAR(); \
    Ls = MFMA16(ones, as_frag(p0), Ls); EX(SC, 13); EX(SC, 14); EX(SC, 15); CV(p1, SC, 4); CV(p1, SC, 5); CV(p1, SC, 6); CV(p1, SC, 7); EX(SN, 0); EX(SN, 1); PIN(SC); PIN(SN); PIN(p1); SBAR(); \
    } while (0)

__global__ __launch_bounds__(512, 2) void k_attn(char* __restrict__ ws) {
    __shared__ __attribute__((aligned(1024))) char smem[A_LDS];
    const lds_cptr L = (lds_cptr)smem;
    const unsigned lds0 = (unsigned)(uintptr_t)(lds_ptr)smem;
    const int lane = threadIdx.x & 63, w = rfl(threadIdx.x >> 6);
    const int xcd = blockIdx.x & 7, li = blockIdx.x >> 3;
    const int head = xcd * 4 + (li & 3), tg = li >> 2, tile = tg * 8 + w;
    const srd_t wsr = make_srd(ws, WS_END);
    const int r = lane & 31, h = lane >> 5;
    const unsigned lv = (unsigned)lane * 16u;

    STAMP(1, 0);
    const int bt = tg * 8 - 8 + 2 * w;
    const unsigned kvb = WS_KVB + (unsigned)(head * 64 + (bt < 0 ? bt + 8 : bt)) * 4096u;
#pragma unroll
    for (int i = 0; i < 8; ++i) dma16(wsr, lv, kvb + (unsigned)i * 1024u, lds0 + (w * 8 + i) * 1024);
#pragma unroll
    for (int i = 0; i < 4; ++i) dma16(wsr, lv, WS_KVA + (unsigned)(w * 4 + i) * 1024u, lds0 + 2 * A_SLOT + (w * 4 + i) * 1024);
    const unsigned qoff = (unsigned)(head * 64 + tile) * 2048u + lv;
    bf16x8 q0 = as_frag(ld16(wsr, WS_QB + qoff)), q1 = as_frag(ld16(wsr, WS_QB + qoff + 1024));
    const bf16x8 qa0 = as_frag(ld16(wsr, WS_QA + qoff)), qa1 = as_frag(ld16(wsr, WS_QA + qoff + 1024));
    const float gate = __uint_as_float(__builtin_amdgcn_raw_buffer_load_b32(wsr, WS_G + (tile * 32 + r) * 4, 0, 0));
    const unsigned one2 = (((lane & 3) == 0) && (((lane >> 4) & 1) == ((lane >> 2) & 1))) ? 0x3F803F80u : 0u;
    const bf16x8 ones = as_frag((u32x4){one2, one2, one2, one2});

    f32x16 O, S0, S1; f32x4 Ls; bf16x8 kf0, kf1, vf0, vf1; u32x4 p0, p1;
    const unsigned obase = WS_O + (unsigned)tile * 131072u + (unsigned)head * 2048u + lv;

    WAITV(0); BAR();
    STAMP(1, 1);
    {
        const int nt = (tg == 0) ? w + 1 : 9;
        const lds_cptr kv = L + lv + (unsigned)(w + 8) * 4096u;
        kf0 = lds_frag(kv); kf1 = lds_frag(kv + 1024);
        S0 = cblock(0.f, 1, r, h);
        S0 = MFMA32(kf0, q0, S0); S0 = MFMA32(kf1, q1, S0);
        const float m = half_max(S0);
        f32x16 negm = cblock(-m, 0, r, h);
        f32x16 cfar = cblock(nt == 9 ? -m : NEG_INF, 2, r, h);
#pragma unroll
        for (int e = 0; e < 16; ++e) S0[e] -= m;
        kf0 = lds_frag(kv - 4096); kf1 = lds_frag(kv - 4096 + 1024);
        vf0 = lds_frag(kv + 2048); vf1 = as_frag((u32x4){0, 0, 0, 0}); p1 = (u32x4){0, 0, 0, 0};
        O = f32x16{}; Ls = f32x4{};
        EX(S0, 0); EX(S0, 1); SBAR();
#define BNEG(i) if (nt == (i)) negm = cblock(NEG_INF, 0, r, h)
        BNEG(1); CYCLE(S0, S1, negm, kv - 2 * 4096, kv, kv - 1 * 4096);
        BNEG(2); CYCLE(S1, S0, negm, kv - 3 * 4096, kv - 1 * 4096, kv - 2 * 4096);
        BNEG(3); CYCLE(S0, S1, negm, kv - 4 * 4096, kv - 2 * 4096, kv - 3 * 4096);
        BNEG(4); CYCLE(S1, S0, negm, kv - 5 * 4096, kv - 3 * 4096, kv - 4 * 4096);
        BNEG(5); CYCLE(S0, S1, negm, kv - 6 * 4096, kv - 4 * 4096, kv - 5 * 4096);
        BNEG(6); CYCLE(S1, S0, negm, kv - 7 * 4096, kv - 5 * 4096, kv - 6 * 4096);
        BNEG(7); CYCLE(S0, S1, negm, kv - 8 * 4096, kv - 6 * 4096, kv - 7 * 4096);
        CYCLE(S1, S0, cfar, kv - 8 * 4096, kv - 7 * 4096, kv - 8 * 4096);
        CYCLE(S0, S1, negm, kv - 8 * 4096, kv - 8 * 4096, kv - 8 * 4096);
#undef BNEG
        O = MFMA32(vf1, as_frag(p1), O); Ls = MFMA16(ones, as_frag(p1), Ls);
        if (attn_bad(O, Ls)) {
            const int jd = head * 64 + tile, jf = (nt == 9) ? jd - 8 : -1;
            attn_safe(wsr, WS_KVB, jd - (nt - 1), jd, jd, jf, q0, q1, ones, lane, O, Ls);
        }
        const float f = (1.0f - gate) / Ls[0];
#pragma unroll
        for (int e = 0; e < 16; ++e) O[e] *= f;
        st16(wsr, obase + 65536, pack8(O, 0)); st16(wsr, obase + 65536 + 1024, pack8(O, 1));
    }
    {
        STAMP(1, 2);
        q0 = qa0; q1 = qa1;
        asm volatile("s_waitcnt lgkmcnt(0)\n\ts_barrier" ::: "memory");
#define A_ISSUE(c, sl) do { _Pragma("unroll") for (int i_ = 0; i_ < 4; ++i_) \
            dma16(wsr, lv, WS_KVA + (unsigned)(c) * 32768u + (unsigned)(w * 4 + i_) * 1024u, lds0 + (sl) + (w * 4 + i_) * 1024); } while (0)
        A_ISSUE(1, 0);
        STAMP(1, 3);
        unsigned sc = 2 * A_SLOT, sn = 0, sf = A_SLOT;
        lds_cptr kv = L + lv + sc;
        kf0 = lds_frag(kv); kf1 = lds_frag(kv + 1024);
        S0 = MFMA32(kf0, q0, f32x16{}); S0 = MFMA32(kf1, q1, S0);
        const float m = half_max(S0);
        f32x16 negm = cblock(-m, 0, r, h);
#pragma unroll
        for (int e = 0; e < 16; ++e) S0[e] -= m;
        kf0 = lds_frag(kv + 4096); kf1 = lds_frag(kv + 4096 + 1024);
        vf0 = lds_frag(kv + 2048); vf1 = as_frag((u32x4){0, 0, 0, 0}); p1 = (u32x4){0, 0, 0, 0};
        O = f32x16{}; Ls = f32x4{};
        EX(S0, 0); EX(S0, 1); SBAR();
        for (int c = 0; c < 8; ++c) {
            const lds_cptr kn = L + lv + sn;
            CYCLE(S0, S1, negm, kv + 2 * 4096, kv + 0 * 4096, kv + 1 * 4096);
            CYCLE(S1, S0, negm, kv + 3 * 4096, kv + 1 * 4096, kv + 2 * 4096);
            CYCLE(S0, S1, negm, kv + 4 * 4096, kv + 2 * 4096, kv + 3 * 4096);
            CYCLE(S1, S0, negm, kv + 5 * 4096, kv + 3 * 4096, kv + 4 * 4096);
            CYCLE(S0, S1, negm, kv + 6 * 4096, kv + 4 * 4096, kv + 5 * 4096);
            CYCLE(S1, S0, negm, kv + 7 * 4096, kv + 5 * 4096, kv + 6 * 4096);
            WAITV(0); BAR();
            if (c < 6) A_ISSUE(c + 2, sf);
            CYCLE(S0, S1, negm, kn, kv + 6 * 4096, kv + 7 * 4096);
            CYCLE(S1, S0, negm, kn + 4096, kv + 7 * 4096, kn);
            kv = kn; const unsigned t_ = sc; sc = sn; sn = sf; sf = t_;
        }
#undef A_ISSUE
        STAMP(1, 4);
        O = MFMA32(vf1, as_frag(p1), O); Ls = MFMA16(ones, as_frag(p1), Ls);
        if (attn_bad(O, Ls)) attn_safe(wsr, WS_KVA, 0, 63, -1, -1, q0, q1, ones, lane, O, Ls);
        const float f = gate / Ls[0];
#pragma unroll
        for (int e = 0; e < 16; ++e) O[e] *= f;
        st16(wsr, obase, pack8(O, 0)); st16(wsr, obase + 1024, pack8(O, 1));
        STAMP(1, 5);
    }
}

constexpr int O_STAGE = 32768 + 16384, O_LDS = 3 * O_STAGE;

__global__ __launch_bounds__(512, 2) void k_out(const __hip_bfloat16* __restrict__ woA, const __hip_bfloat16* __restrict__ woB,
                                                char* __restrict__ ws, __hip_bfloat16* __restrict__ out) {
    __shared__ __attribute__((aligned(1024))) char smem[O_LDS];
    const lds_cptr L = (lds_cptr)smem;
    const unsigned lds0 = (unsigned)(uintptr_t)(lds_ptr)smem;
    const int lane = threadIdx.x & 63, wid = rfl(threadIdx.x >> 6);
    const int xcd = blockIdx.x & 7, li = blockIdx.x >> 3;
    const int tb = (xcd >> 1) * 4 + (li & 3), nb = (xcd & 1) * 8 + (li >> 2);
    STAMP(2, 0);
    if (wid < 4) {
        const int wm = wid >> 1, wn = wid & 1;
        const int r = lane & 31, h = lane >> 5, g1 = (lane >> 4) & 1, q = (lane & 15) >> 2, pp = lane & 3;
        const unsigned oo = (unsigned)(wm * 2 * 8192 + lane * 16);
        const unsigned wo = (unsigned)(32768 + (4 * h + q) * 128 + (((wn * 4 + 2 * g1 + (pp >> 1)) ^ ((q >> 1) << 2)) << 4) + 8 * (pp & 1));
        f32x16 acc0 = f32x16{}, acc1 = f32x16{};
        bf16x8 of[2][4]; s16x4 wl[2][2], wh[2][2];
#define O_RD(b, sl, kk) do { _Pragma("unroll") for (int u_ = 0; u_ < 2; ++u_) { \
            of[b][2 * u_] = lds_frag(L + (sl) + oo + ((kk) + u_) * 1024); of[b][2 * u_ + 1] = lds_frag(L + (sl) + oo + 8192 + ((kk) + u_) * 1024); \
            wl[b][u_] = lds_tr(L + (sl) + wo + ((kk) + u_) * 2048); wh[b][u_] = lds_tr(L + (sl) + wo + ((kk) + u_) * 2048 + 1024); } } while (0)
#define O_MMA(b) do { _Pragma("unroll") for (int u_ = 0; u_ < 2; ++u_) { const bf16x8 wf_ = join_tr(wl[b][u_], wh[b][u_]); \
            acc0 = MFMA32(wf_, of[b][2 * u_], acc0); acc1 = MFMA32(wf_, of[b][2 * u_ + 1], acc1); } } while (0)
#define O_ORDER() do { _Pragma("unroll") for (int i_ = 0; i_ < 2; ++i_) { SGB(0x008, 1); SGB(0x100, 4); } SGB(0x008, 2); SBAR(); } while (0)
        unsigned slot = 0;
        BAR();
        STAMP(2, 1);
        O_RD(0, slot, 0); SBAR();
        for (int t = 0; t < 16; ++t) {
            O_RD(1, slot, 2); O_MMA(0); O_ORDER();
            O_RD(0, slot, 4); O_MMA(1); O_ORDER();
            O_RD(1, slot, 6); O_MMA(0); O_ORDER();
            if (t < 15) {
                WAITL_BAR();
                slot = (slot == 2 * O_STAGE) ? 0u : slot + O_STAGE;
                O_RD(0, slot, 0); O_MMA(1); O_ORDER();
            } else { O_MMA(1); SBAR(); }
        }
#undef O_RD
#undef O_MMA
#undef O_ORDER
        STAMP(2, 2);
        const srd_t os = make_srd(out, SEQ * DM * 2);
#pragma unroll
        for (int i = 0; i < 2; ++i) {
            const f32x16& a = i ? acc1 : acc0;
            const unsigned row = (unsigned)(tb * 128 + wm * 64 + i * 32 + r);
            const unsigned base = row * 2048u + (unsigned)(nb * 128 + wn * 64 + h * 16);
#pragma unroll
            for (int g = 0; g < 4; g += 2) {
                unsigned ax = pack2(a[4 * g], a[4 * g + 1]), ay = pack2(a[4 * g + 2], a[4 * g + 3]);
                unsigned bx = pack2(a[4 * g + 4], a[4 * g + 5]), by = pack2(a[4 * g + 6], a[4 * g + 7]);
                auto rx = __builtin_amdgcn_permlane32_swap(ax, bx, false, false);
                auto ry = __builtin_amdgcn_permlane32_swap(ay, by, false, false);
                st16(os, base + g * 16, (u32x4){rx[0], ry[0], rx[1], ry[1]});
            }
        }
        STAMP(2, 3);
    } else {
        const int p = wid - 4;
        const srd_t wsr = make_srd(ws, WS_END);
        const srd_t wa = make_srd(woA, DM * DM * 2), wb = make_srd(woB, DM * DM * 2);
        const unsigned ov = (unsigned)lane * 16u;
        unsigned wv[4];
#pragma unroll
        for (int i = 0; i < 4; ++i) { const int row = p * 32 + i * 8 + (lane >> 3); wv[i] = (unsigned)(row * 2048 + nb * 128 + (((lane & 7) ^ (((lane >> 4) & 1) << 2)) << 4)); }
        const unsigned obase = WS_O + (unsigned)(tb * 4 + p) * 131072u;
#define O_ISSUE(t, sl) do { const unsigned d_ = lds0 + (sl); \
        _Pragma("unroll") for (int i_ = 0; i_ < 8; ++i_) dma16(wsr, ov, obase + (unsigned)(t) * 8192u + i_ * 1024, d_ + p * 8192 + i_ * 1024); \
        if ((t) < 8) { _Pragma("unroll") for (int i_ = 0; i_ < 4; ++i_) dma16(wa, wv[i_], (unsigned)(t) * 262144u, d_ + 32768 + p * 4096 + i_ * 1024); } \
        else { _Pragma("unroll") for (int i_ = 0; i_ < 4; ++i_) dma16(wb, wv[i_], (unsigned)((t) - 8) * 262144u, d_ + 32768 + p * 4096 + i_ * 1024); } } while (0)
        O_ISSUE(0, 0); O_ISSUE(1, O_STAGE);
        WAITV(12); BAR();
        unsigned s2 = 2 * O_STAGE;
        for (int t = 0; t < 15; ++t) {
            if (t < 14) { O_ISSUE(t + 2, s2); s2 = (s2 == 2 * O_STAGE) ? 0u : s2 + O_STAGE; WAITV(12); } else { WAITV(0); }
            BAR();
        }
#undef O_ISSUE
    }
}

extern "C" void kernel_launch(void* const* d_in, const int* in_sizes, int n_in, void* d_out, int out_size, void* d_ws, size_t ws_size,
                              hipStream_t stream) {
    const __hip_bfloat16* xA = (const __hip_bfloat16*)d_in[0];
    const __hip_bfloat16* wq = (const __hip_bfloat16*)d_in[1];
    const __hip_bfloat16* wk = (const __hip_bfloat16*)d_in[2];
    const __hip_bfloat16* wv = (const __hip_bfloat16*)d_in[3];
    const __hip_bfloat16* woA = (const __hip_bfloat16*)d_in[4];
    const __hip_bfloat16* xB = xA;
    const __hip_bfloat16* wqkv = (const __hip_bfloat16*)d_in[6];
    const __hip_bfloat16* woB = (const __hip_bfloat16*)d_in[7];
    const __hip_bfloat16* gate_w = (const __hip_bfloat16*)d_in[8];
    char* ws = (char*)d_ws;
    k_proj<<<256, 512, 0, stream>>>(xA, wq, wk, wv, xB, wqkv, gate_w, ws);
    k_attn<<<256, 512, 0, stream>>>(ws);
    k_out<<<256, 512, 0, stream>>>(woA, woB, ws, (__hip_bfloat16*)d_out);
}
```

```cpp
#include <hip/hip_runtime.h>
#include <hip/hip_bf16.h>
#include <stdint.h>

#define DI __device__ __forceinline__
typedef __attribute__((ext_vector_type(8))) __bf16 bf16x8;
typedef __attribute__((ext_vector_type(2))) __bf16 bf16x2;
typedef __attribute__((ext_vector_type(4))) short s16x4;
typedef __attribute__((ext_vector_type(2))) float f32x2;
typedef __attribute__((ext_vector_type(4))) float f32x4;
typedef __attribute__((ext_vector_type(16))) float f32x16;
typedef __attribute__((ext_vector_type(2))) unsigned u32x2;
typedef __attribute__((ext_vector_type(4))) unsigned u32x4;
typedef __attribute__((address_space(3))) const char* lds_cptr;
typedef __attribute__((address_space(3))) char* lds_ptr;
typedef __amdgpu_buffer_rsrc_t srd_t;

constexpr int SEQ = 2048, DM = 1024;
constexpr float QK_SCALE = 0.17677669529663687f * 1.4426950408889634f;

constexpr unsigned WS_KVA = 0;
constexpr unsigned WS_G = 256u << 10;
constexpr unsigned WS_EPOCH = 272u << 10;
constexpr unsigned WS_FLAG1 = WS_EPOCH + 4096;
constexpr unsigned WS_FLAGB = WS_EPOCH + 1024;
constexpr unsigned WS_FLAG2 = WS_EPOCH + 16384;
constexpr unsigned WS_KVB = 1u << 20;
constexpr unsigned WS_O = 9u << 20;
constexpr unsigned WS_END = 17u << 20;

#define MFMA32(a, b, c) __builtin_amdgcn_mfma_f32_32x32x16_bf16((a), (b), (c), 0, 0, 0)
#define MFMA16(a, b, c) __builtin_amdgcn_mfma_f32_16x16x32_bf16((a), (b), (c), 0, 0, 0)
#define SBAR() __builtin_amdgcn_sched_barrier(0)
#define SGB(mask, n) __builtin_amdgcn_sched_group_barrier((mask), (n), 0)
#define BAR() asm volatile("s_barrier" ::: "memory")
#define WAITV_(n) asm volatile("s_waitcnt vmcnt(" #n ")" ::: "memory")
#define WAITV(n) WAITV_(n)
#define WAITL_BAR() asm volatile("s_waitcnt lgkmcnt(0)\n\ts_barrier" ::: "memory")
#define WAITVL_BAR() asm volatile("s_waitcnt vmcnt(0) lgkmcnt(0)\n\ts_barrier" ::: "memory")
#define PIN(x) asm volatile("" : "+v"(x))


DI unsigned rfl(unsigned v) { return __builtin_amdgcn_readfirstlane(v); }
DI srd_t make_srd(const void* p, unsigned bytes) { return __builtin_amdgcn_make_buffer_rsrc((void*)p, (short)0, (int)bytes, 0x00020000); }
DI unsigned pack2(float lo, float hi) { f32x2 v = {lo, hi}; return __builtin_bit_cast(unsigned, __builtin_convertvector(v, bf16x2)); }
DI u32x4 pack8(const f32x16& a, int s) {
    u32x4 r; r[0] = pack2(a[8 * s + 0], a[8 * s + 1]); r[1] = pack2(a[8 * s + 2], a[8 * s + 3]);
    r[2] = pack2(a[8 * s + 4], a[8 * s + 5]); r[3] = pack2(a[8 * s + 6], a[8 * s + 7]); return r; }
DI bf16x8 as_frag(const u32x4& v) { return __builtin_bit_cast(bf16x8, v); }
DI bf16x8 lds_frag(lds_cptr p) { return *(const __attribute__((address_space(3))) bf16x8*)p; }
DI u32x4 lds_u4(lds_cptr p) { return *(const __attribute__((address_space(3))) u32x4*)p; }
DI void lds_st4(lds_cptr p, const u32x4& v) { *(__attribute__((address_space(3))) u32x4*)p = v; }
DI s16x4 lds_tr(lds_cptr p) {
    typedef short v4i16_t __attribute__((ext_vector_type(4)));
    return __builtin_amdgcn_ds_read_tr16_b64_v4i16((__attribute__((address_space(3))) v4i16_t*)p); }
DI bf16x8 join_tr(const s16x4& lo, const s16x4& hi) { return __builtin_bit_cast(bf16x8, __builtin_shufflevector(lo, hi, 0, 1, 2, 3, 4, 5, 6, 7)); }
DI void dma16(srd_t srd, unsigned voff, unsigned soff, unsigned m0v) {
    asm volatile("s_mov_b32 m0, %3\n\ts_nop 0\n\tbuffer_load_dwordx4 %0, %1, %2 offen lds" :: "v"(voff), "s"(srd), "s"(soff), "s"(m0v) : "m0", "memory"); }
constexpr int SC1 = 16;
DI void st16(srd_t srd, unsigned voff, const u32x4& v) { __builtin_amdgcn_raw_buffer_store_b128(v, srd, voff, 0, SC1); }
DI u32x4 ld16(srd_t srd, unsigned voff) { return __builtin_amdgcn_raw_buffer_load_b128(srd, voff, 0, SC1); }
DI void poll_flags(srd_t wsr, unsigned voff, unsigned tag) {
#pragma unroll 1
    for (int it = 0; it < 200000; ++it) {
        u32x4 v;
        asm volatile("buffer_load_dwordx4 %0, %1, %2, 0 offen sc1\n\ts_waitcnt vmcnt(0)" : "=&v"(v) : "v"(voff), "s"(wsr) : "memory");
        if (__all(v[0] == tag && v[1] == tag && v[2] == tag && v[3] == tag)) return;
        __builtin_amdgcn_s_sleep(1);
    }
}

DI void poll_word(srd_t wsr, unsigned voff, unsigned tag) {
#pragma unroll 1
    for (int it = 0; it < 200000; ++it) {
        unsigned v;
        asm volatile("buffer_load_dword %0, %1, %2, 0 offen sc1\n\ts_waitcnt vmcnt(0)" : "=&v"(v) : "v"(voff), "s"(wsr) : "memory");
        if (__all(v == tag)) return;
        __builtin_amdgcn_s_sleep(1);
    }
}

constexpr float NEG_INF = -__builtin_inff();
DI float half_max(const f32x16& s) {
    float a = fmaxf(fmaxf(s[0], s[1]), s[2]), b = fmaxf(fmaxf(s[3], s[4]), s[5]);
    a = fmaxf(fmaxf(a, s[6]), s[7]); b = fmaxf(fmaxf(b, s[8]), s[9]);
    a = fmaxf(fmaxf(a, s[10]), s[11]); b = fmaxf(fmaxf(b, s[12]), s[13]);
    a = fmaxf(fmaxf(a, s[14]), s[15]); a = fmaxf(a, b);
    auto rr = __builtin_amdgcn_permlane32_swap(__float_as_uint(a), __float_as_uint(a), false, false);
    return fmaxf(__uint_as_float(rr[0]), __uint_as_float(rr[1])); }
DI f32x16 cblock(float base, int mode, int r, int h) {
    f32x16 c; const int hi = (mode == 1 ? r : 63) - 4 * h, lo = (mode == 2 ? r : -1) - 4 * h;
#pragma unroll
    for (int e = 0; e < 16; ++e) { const int k = (e & 3) + 8 * (e >> 2); c[e] = (k <= hi && k > lo) ? base : NEG_INF; }
    return c; }
DI void attn_safe(srd_t wsr, unsigned kvbase, int j0, int j1, int jdiag, int jfar, const bf16x8& q0, const bf16x8& q1, const bf16x8& ones, int lane, f32x16& O, f32x4& Lsum) {
    const int r = lane & 31, h = lane >> 5;
    float m = NEG_INF;
    for (int j = j0; j <= j1; ++j) {
        const unsigned a = kvbase + (unsigned)j * 4096u + lane * 16;
        const bf16x8 k0 = as_frag(ld16(wsr, a)), k1 = as_frag(ld16(wsr, a + 1024));
        f32x16 s = cblock(0.f, j == jdiag ? 1 : (j == jfar ? 2 : 0), r, h);
        s = MFMA32(k0, q0, s); s = MFMA32(k1, q1, s);
        m = fmaxf(m, half_max(s));
    }
    O = f32x16{}; Lsum = f32x4{};
    for (int j = j0; j <= j1; ++j) {
        const unsigned a = kvbase + (unsigned)j * 4096u + lane * 16;
        const bf16x8 k0 = as_frag(ld16(wsr, a)), k1 = as_frag(ld16(wsr, a + 1024)), v0 = as_frag(ld16(wsr, a + 2048)), v1 = as_frag(ld16(wsr, a + 3072));
        f32x16 s = cblock(-m, j == jdiag ? 1 : (j == jfar ? 2 : 0), r, h);
        s = MFMA32(k0, q0, s); s = MFMA32(k1, q1, s);
#pragma unroll
        for (int e = 0; e < 16; ++e) s[e] = __builtin_amdgcn_exp2f(s[e]);
        const bf16x8 p0 = as_frag(pack8(s, 0)), p1 = as_frag(pack8(s, 1));
        O = MFMA32(v0, p0, O); O = MFMA32(v1, p1, O);
        Lsum = MFMA16(ones, p0, Lsum); Lsum = MFMA16(ones, p1, Lsum);
    }
}
DI bool attn_bad(const f32x16& O, const f32x4& Lsum) {
    float a = fabsf(Lsum[0]);
#pragma unroll
    for (int e = 0; e < 16; ++e) a = fmaxf(a, fabsf(O[e]));
    bool bad = !(a < 1.0e37f);
#pragma unroll
    for (int e = 0; e < 16; ++e) bad |= (O[e] != O[e]);
    bad |= !(Lsum[0] > 1.0e-18f);
    return __any(bad) != 0; }

#define ATTN_A0_ASM \
    "s_nop 1\n\t" \
    "ds_read_b128 v[200:203], %[k0a] offset:0\n\t" \
    "ds_read_b128 v[204:207], %[k0a] offset:1024\n\t" \
    "v_mov_b32 v196, 0\n\t" \
    "v_mov_b32 v197, 0\n\t" \
    "v_mov_b32 v198, 0\n\t" \
    "v_mov_b32 v199, 0\n\t" \
    "v_mov_b32 v212, 0\n\t" \
    "v_mov_b32 v213, 0\n\t" \
    "v_mov_b32 v214, 0\n\t" \
    "v_mov_b32 v215, 0\n\t" \
    "s_waitcnt lgkmcnt(0)\n\t" \
    "v_mfma_f32_32x32x16_bf16 v[128:143], v[200:203], %[qx0], 0\n\t" \
    "v_mfma_f32_32x32x16_bf16 v[128:143], v[204:207], %[qx1], v[128:143]\n\t" \
    "v_mfma_f32_32x32x16_bf16 v[144:159], v[200:203], %[qy0], 0\n\t" \
    "v_mfma_f32_32x32x16_bf16 v[144:159], v[204:207], %[qy1], v[144:159]\n\t" \
    "ds_read_b128 v[200:203], %[cur] offset:0\n\t" \
    "ds_read_b128 v[204:207], %[cur] offset:1024\n\t" \
    "ds_read_b128 v[208:211], %[cur] offset:2048\n\t" \
    "s_nop 8\n\t" \
    "v_max3_f32 v216, v128, v129, v130\n\t" \
    "v_max3_f32 v217, v131, v132, v133\n\t" \
    "v_max3_f32 v216, v216, v134, v135\n\t" \
    "v_max3_f32 v217, v217, v136, v137\n\t" \
    "v_max3_f32 v216, v216, v138, v139\n\t" \
    "v_max3_f32 v217, v217, v140, v141\n\t" \
    "v_max3_f32 v216, v216, v142, v143\n\t" \
    "v_max_f32 v216, v216, v217\n\t" \
    "v_mov_b32 v217, v216\n\t" \
    "s_nop 1\n\t" \
    "v_permlane32_swap_b32 v216, v217\n\t" \
    "s_nop 1\n\t" \
    "v_max_f32 v216, v216, v217\n\t" \
    "v_sub_f32 v160, 0, v216\n\t" \
    "v_sub_f32 v161, 0, v216\n\t" \
    "v_sub_f32 v162, 0, v216\n\t" \
    "v_sub_f32 v163, 0, v216\n\t" \
    "v_sub_f32 v164, 0, v216\n\t" \
    "v_sub_f32 v165, 0, v216\n\t" \
    "v_sub_f32 v166, 0, v216\n\t" \
    "v_sub_f32 v167, 0, v216\n\t" \
    "v_sub_f32 v168, 0, v216\n\t" \
    "v_sub_f32 v169, 0, v216\n\t" \
    "v_sub_f32 v170, 0, v216\n\t" \
    "v_sub_f32 v171, 0, v216\n\t" \
    "v_sub_f32 v172, 0, v216\n\t" \
    "v_sub_f32 v173, 0, v216\n\t" \
    "v_sub_f32 v174, 0, v216\n\t" \
    "v_sub_f32 v175, 0, v216\n\t" \
    "s_waitcnt lgkmcnt(0)\n\t" \
    "s_nop 0\n\t" \
    "v_mfma_f32_32x32x16_bf16 v[128:143], v[200:203], %[qx0], v[160:175]\n\t" \
    "v_mfma_f32_32x32x16_bf16 v[128:143], v[204:207], %[qx1], v[128:143]\n\t" \
    "v_max3_f32 v216, v144, v145, v146\n\t" \
    "v_max3_f32 v217, v147, v148, v149\n\t" \
    "v_max3_f32 v216, v216, v150, v151\n\t" \
    "v_max3_f32 v217, v217, v152, v153\n\t" \
    "v_max3_f32 v216, v216, v154, v155\n\t" \
    "v_max3_f32 v217, v217, v156, v157\n\t" \
    "v_max3_f32 v216, v216, v158, v159\n\t" \
    "v_max_f32 v216, v216, v217\n\t" \
    "v_mov_b32 v217, v216\n\t" \
    "s_nop 1\n\t" \
    "v_permlane32_swap_b32 v216, v217\n\t" \
    "s_nop 1\n\t" \
    "v_max_f32 v216, v216, v217\n\t" \
    "v_sub_f32 v176, 0, v216\n\t" \
    "v_sub_f32 v177, 0, v216\n\t" \
    "v_sub_f32 v178, 0, v216\n\t" \
    "v_sub_f32 v179, 0, v216\n\t" \
    "v_sub_f32 v180, 0, v216\n\t" \
    "v_sub_f32 v181, 0, v216\n\t" \
    "v_sub_f32 v182, 0, v216\n\t" \
    "v_sub_f32 v183, 0, v216\n\t" \
    "v_sub_f32 v184, 0, v216\n\t" \
    "v_sub_f32 v185, 0, v216\n\t" \
    "v_sub_f32 v186, 0, v216\n\t" \
    "v_sub_f32 v187, 0, v216\n\t" \
    "v_sub_f32 v188, 0, v216\n\t" \
    "v_sub_f32 v189, 0, v216\n\t" \
    "v_sub_f32 v190, 0, v216\n\t" \
    "v_sub_f32 v191, 0, v216\n\t" \
    "v_exp_f32 v128, v128\n\t" \
    "v_exp_f32 v129, v129\n\t" \
    "s_mov_b32 %[cnt], 0\n\t" \
    "1:\n\t" \
    "v_mfma_f32_32x32x16_bf16 %[OY], v[212:215], v[196:199], %[OY]\n\t" \
    "ds_read_b128 v[212:215], %[cur] offset:3072\n\t" \
    "v_exp_f32 v130, v130\n\t" \
    "v_exp_f32 v131, v131\n\t" \
    "v_exp_f32 v132, v132\n\t" \
    "v_mfma_f32_16x16x32_bf16 %[LY], %[ones], v[196:199], %[LY]\n\t" \
    "v_exp_f32 v133, v133\n\t" \
    "v_mfma_f32_32x32x16_bf16 v[144:159], v[200:203], %[qy0], v[176:191]\n\t" \
    "ds_read_b128 v[200:203], %[cur] offset:4096\n\t" \
    "v_exp_f32 v134, v134\n\t" \
    "v_exp_f32 v135, v135\n\t" \
    "v_cvt_pk_bf16_f32 v192, v128, v129\n\t" \
    "v_cvt_pk_bf16_f32 v193, v130, v131\n\t" \
    "v_mfma_f32_32x32x16_bf16 v[144:159], v[204:207], %[qy1], v[144:159]\n\t" \
    "ds_read_b128 v[204:207], %[cur] offset:5120\n\t" \
    "v_cvt_pk_bf16_f32 v194, v132, v133\n\t" \
    "v_cvt_pk_bf16_f32 v195, v134, v135\n\t" \
    "v_exp_f32 v136, v136\n\t" \
    "v_exp_f32 v137, v137\n\t" \
    "s_waitcnt lgkmcnt(3)\n\t" \
    "v_mfma_f32_32x32x16_bf16 %[OX], v[208:211], v[192:195], %[OX]\n\t" \
    "v_exp_f32 v138, v138\n\t" \
    "v_exp_f32 v139, v139\n\t" \
    "v_exp_f32 v140, v140\n\t" \
    "v_mfma_f32_16x16x32_bf16 %[LX], %[ones], v[192:195], %[LX]\n\t" \
    "v_exp_f32 v141, v141\n\t" \
    "v_exp_f32 v142, v142\n\t" \
    "v_exp_f32 v143, v143\n\t" \
    "v_cvt_pk_bf16_f32 v196, v136, v137\n\t" \
    "v_cvt_pk_bf16_f32 v197, v138, v139\n\t" \
    "v_cvt_pk_bf16_f32 v198, v140, v141\n\t" \
    "v_cvt_pk_bf16_f32 v199, v142, v143\n\t" \
    "v_exp_f32 v144, v144\n\t" \
    "v_exp_f32 v145, v145\n\t" \
    "s_waitcnt lgkmcnt(0)\n\t" \
    "v_mfma_f32_32x32x16_bf16 %[OX], v[212:215], v[196:199], %[OX]\n\t" \
    "v_exp_f32 v146, v146\n\t" \
    "v_exp_f32 v147, v147\n\t" \
    "v_exp_f32 v148, v148\n\t" \
    "v_mfma_f32_16x16x32_bf16 %[LX], %[ones], v[196:199], %[LX]\n\t" \
    "v_exp_f32 v149, v149\n\t" \
    "v_mfma_f32_32x32x16_bf16 v[128:143], v[200:203], %[qx0], v[160:175]\n\t" \
    "v_exp_f32 v150, v150\n\t" \
    "v_exp_f32 v151, v151\n\t" \
    "v_cvt_pk_bf16_f32 v192, v144, v145\n\t" \
    "v_cvt_pk_bf16_f32 v193, v146, v147\n\t" \
    "v_mfma_f32_32x32x16_bf16 v[128:143], v[204:207], %[qx1], v[128:143]\n\t" \
    "v_cvt_pk_bf16_f32 v194, v148, v149\n\t" \
    "v_cvt_pk_bf16_f32 v195, v150, v151\n\t" \
    "v_exp_f32 v152, v152\n\t" \
    "v_exp_f32 v153, v153\n\t" \
    "v_mfma_f32_32x32x16_bf16 %[OY], v[208:211], v[192:195], %[OY]\n\t" \
    "ds_read_b128 v[208:211], %[cur] offset:6144\n\t" \
    "v_exp_f32 v154, v154\n\t" \
    "v_exp_f32 v155, v155\n\t" \
    "v_exp_f32 v156, v156\n\t" \
    "v_mfma_f32_16x16x32_bf16 %[LY], %[ones], v[192:195], %[LY]\n\t" \
    "v_exp_f32 v157, v157\n\t" \
    "v_exp_f32 v158, v158\n\t" \
    "v_exp_f32 v159, v159\n\t" \
    "v_cvt_pk_bf16_f32 v196, v152, v153\n\t" \
    "v_cvt_pk_bf16_f32 v197, v154, v155\n\t" \
    "v_cvt_pk_bf16_f32 v198, v156, v157\n\t" \
    "v_cvt_pk_bf16_f32 v199, v158, v159\n\t" \
    "v_exp_f32 v128, v128\n\t" \
    "v_exp_f32 v129, v129\n\t" \
    "v_mfma_f32_32x32x16_bf16 %[OY], v[212:215], v[196:199], %[OY]\n\t" \
    "ds_read_b128 v[212:215], %[cur] offset:7168\n\t" \
    "v_exp_f32 v130, v130\n\t" \
    "v_exp_f32 v131, v131\n\t" \
    "v_exp_f32 v132, v132\n\t" \
    "v_mfma_f32_16x16x32_bf16 %[LY], %[ones], v[196:199], %[LY]\n\t" \
    "v_exp_f32 v133, v133\n\t" \
    "v_mfma_f32_32x32x16_bf16 v[144:159], v[200:203], %[qy0], v[176:191]\n\t" \
    "ds_read_b128 v[200:203], %[cur] offset:8192\n\t" \
    "v_exp_f32 v134, v134\n\t" \
    "v_exp_f32 v135, v135\n\t" \
    "v_cvt_pk_bf16_f32 v192, v128, v129\n\t" \
    "v_cvt_pk_bf16_f32 v193, v130, v131\n\t" \
    "v_mfma_f32_32x32x16_bf16 v[144:159], v[204:207], %[qy1], v[144:159]\n\t" \
    "ds_read_b128 v[204:207], %[cur] offset:9216\n\t" \
    "v_cvt_pk_bf16_f32 v194, v132, v133\n\t" \
    "v_cvt_pk_bf16_f32 v195, v134, v135\n\t" \
    "v_exp_f32 v136, v136\n\t" \
    "v_exp_f32 v137, v137\n\t" \
    "s_waitcnt lgkmcnt(3)\n\t" \
    "v_mfma_f32_32x32x16_bf16 %[OX], v[208:211], v[192:195], %[OX]\n\t" \
    "v_exp_f32 v138, v138\n\t" \
    "v_exp_f32 v139, v139\n\t" \
    "v_exp_f32 v140, v140\n\t" \
    "v_mfma_f32_16x16x32_bf16 %[LX], %[ones], v[192:195], %[LX]\n\t" \
    "v_exp_f32 v141, v141\n\t" \
    "v_exp_f32 v142, v142\n\t" \
    "v_exp_f32 v143, v143\n\t" \
    "v_cvt_pk_bf16_f32 v196, v136, v137\n\t" \
    "v_cvt_pk_bf16_f32 v197, v138, v139\n\t" \
    "v_cvt_pk_bf16_f32 v198, v140, v141\n\t" \
    "v_cvt_pk_bf16_f32 v199, v142, v143\n\t" \
    "v_exp_f32 v144, v144\n\t" \
    "v_exp_f32 v145, v145\n\t" \
    "s_waitcnt lgkmcnt(0)\n\t" \
    "v_mfma_f32_32x32x16_bf16 %[OX], v[212:215], v[196:199], %[OX]\n\t" \
    "v_exp_f32 v146, v146\n\t" \
    "v_exp_f32 v147, v147\n\t" \
    "v_exp_f32 v148, v148\n\t" \
    "v_mfma_f32_16x16x32_bf16 %[LX], %[ones], v[196:199], %[LX]\n\t" \
    "v_exp_f32 v149, v149\n\t" \
    "v_mfma_f32_32x32x16_bf16 v[128:143], v[200:203], %[qx0], v[160:175]\n\t" \
    "v_exp_f32 v150, v150\n\t" \
    "v_exp_f32 v151, v151\n\t" \
    "v_cvt_pk_bf16_f32 v192, v144, v145\n\t" \
    "v_cvt_pk_bf16_f32 v193, v146, v147\n\t" \
    "v_mfma_f32_32x32x16_bf16 v[128:143], v[204:207], %[qx1], v[128:143]\n\t" \
    "v_cvt_pk_bf16_f32 v194, v148, v149\n\t" \
    "v_cvt_pk_bf16_f32 v195, v150, v151\n\t" \
    "v_exp_f32 v152, v152\n\t" \
    "v_exp_f32 v153, v153\n\t" \
    "v_mfma_f32_32x32x16_bf16 %[OY], v[208:211], v[192:195], %[OY]\n\t" \
    "ds_read_b128 v[208:211], %[cur] offset:10240\n\t" \
    "v_exp_f32 v154, v154\n\t" \
    "v_exp_f32 v155, v155\n\t" \
    "v_exp_f32 v156, v156\n\t" \
    "v_mfma_f32_16x16x32_bf16 %[LY], %[ones], v[192:195], %[LY]\n\t" \
    "v_exp_f32 v157, v157\n\t" \
    "v_exp_f32 v158, v158\n\t" \
    "v_exp_f32 v159, v159\n\t" \
    "v_cvt_pk_bf16_f32 v196, v152, v153\n\t" \
    "v_cvt_pk_bf16_f32 v197, v154, v155\n\t" \
    "v_cvt_pk_bf16_f32 v198, v156, v157\n\t" \
    "v_cvt_pk_bf16_f32 v199, v158, v159\n\t" \
    "v_exp_f32 v128, v128\n\t" \
    "v_exp_f32 v129, v129\n\t" \
    "v_mfma_f32_32x32x16_bf16 %[OY], v[212:215], v[196:199], %[OY]\n\t" \
    "ds_read_b128 v[212:215], %[cur] offset:11264\n\t" \
    "v_exp_f32 v130, v130\n\t" \
    "v_exp_f32 v131, v131\n\t" \
    "v_exp_f32 v132, v132\n\t" \
    "v_mfma_f32_16x16x32_bf16 %[LY], %[ones], v[196:199], %[LY]\n\t" \
    "v_exp_f32 v133, v133\n\t" \
    "v_mfma_f32_32x32x16_bf16 v[144:159], v[200:203], %[qy0], v[176:191]\n\t" \
    "ds_read_b128 v[200:203], %[cur] offset:12288\n\t" \
    "v_exp_f32 v134, v134\n\t" \
    "v_exp_f32 v135, v135\n\t" \
    "v_cvt_pk_bf16_f32 v192, v128, v129\n\t" \
    "v_cvt_pk_bf16_f32 v193, v130, v131\n\t" \
    "v_mfma_f32_32x32x16_bf16 v[144:159], v[204:207], %[qy1], v[144:159]\n\t" \
    "ds_read_b128 v[204:207], %[cur] offset:13312\n\t" \
    "v_cvt_pk_bf16_f32 v194, v132, v133\n\t" \
    "v_cvt_pk_bf16_f32 v195, v134, v135\n\t" \
    "v_exp_f32 v136, v136\n\t" \
    "v_exp_f32 v137, v137\n\t" \
    "s_waitcnt lgkmcnt(3)\n\t" \
    "v_mfma_f32_32x32x16_bf16 %[OX], v[208:211], v[192:195], %[OX]\n\t" \
    "v_exp_f32 v138, v138\n\t" \
    "v_exp_f32 v139, v139\n\t" \
    "v_exp_f32 v140, v140\n\t" \
    "v_mfma_f32_16x16x32_bf16 %[LX], %[ones], v[192:195], %[LX]\n\t" \
    "v_exp_f32 v141, v141\n\t" \
    "v_exp_f32 v142, v142\n\t" \
    "v_exp_f32 v143, v143\n\t" \
    "v_cvt_pk_bf16_f32 v196, v136, v137\n\t" \
    "v_cvt_pk_bf16_f32 v197, v138, v139\n\t" \
    "v_cvt_pk_bf16_f32 v198, v140, v141\n\t" \
    "v_cvt_pk_bf16_f32 v199, v142, v143\n\t" \
    "v_exp_f32 v144, v144\n\t" \
    "v_exp_f32 v145, v145\n\t" \
    "s_waitcnt lgkmcnt(0)\n\t" \
    "v_mfma_f32_32x32x16_bf16 %[OX], v[212:215], v[196:199], %[OX]\n\t" \
    "v_exp_f32 v146, v146\n\t" \
    "v_exp_f32 v147, v147\n\t" \
    "v_exp_f32 v148, v148\n\t" \
    "v_mfma_f32_16x16x32_bf16 %[LX], %[ones], v[196:199], %[LX]\n\t" \
    "v_exp_f32 v149, v149\n\t" \
    "v_mfma_f32_32x32x16_bf16 v[128:143], v[200:203], %[qx0], v[160:175]\n\t" \
    "v_exp_f32 v150, v150\n\t" \
    "v_exp_f32 v151, v151\n\t" \
    "v_cvt_pk_bf16_f32 v192, v144, v145\n\t" \
    "v_cvt_pk_bf16_f32 v193, v146, v147\n\t" \
    "v_mfma_f32_32x32x16_bf16 v[128:143], v[204:207], %[qx1], v[128:143]\n\t" \
    "v_cvt_pk_bf16_f32 v194, v148, v149\n\t" \
    "v_cvt_pk_bf16_f32 v195, v150, v151\n\t" \
    "v_exp_f32 v152, v152\n\t" \
    "v_exp_f32 v153, v153\n\t" \
    "v_mfma_f32_32x32x16_bf16 %[OY], v[208:211], v[192:195], %[OY]\n\t" \
    "ds_read_b128 v[208:211], %[cur] offset:14336\n\t" \
    "v_exp_f32 v154, v154\n\t" \
    "v_exp_f32 v155, v155\n\t" \
    "v_exp_f32 v156, v156\n\t" \
    "v_mfma_f32_16x16x32_bf16 %[LY], %[ones], v[192:195], %[LY]\n\t" \
    "v_exp_f32 v157, v157\n\t" \
    "v_exp_f32 v158, v158\n\t" \
    "v_exp_f32 v159, v159\n\t" \
    "v_cvt_pk_bf16_f32 v196, v152, v153\n\t" \
    "v_cvt_pk_bf16_f32 v197, v154, v155\n\t" \
    "v_cvt_pk_bf16_f32 v198, v156, v157\n\t" \
    "v_cvt_pk_bf16_f32 v199, v158, v159\n\t" \
    "v_exp_f32 v128, v128\n\t" \
    "v_exp_f32 v129, v129\n\t" \
    "v_mfma_f32_32x32x16_bf16 %[OY], v[212:215], v[196:199], %[OY]\n\t" \
    "ds_read_b128 v[212:215], %[cur] offset:15360\n\t" \
    "v_exp_f32 v130, v130\n\t" \
    "v_exp_f32 v131, v131\n\t" \
    "v_exp_f32 v132, v132\n\t" \
    "v_mfma_f32_16x16x32_bf16 %[LY], %[ones], v[196:199], %[LY]\n\t" \
    "v_exp_f32 v133, v133\n\t" \
    "s_waitcnt vmcnt(2)\n\t" \
    "s_barrier\n\t" \
    "s_cmp_lg_u32 %[cnt], 1\n\t" \
    "s_cbranch_scc1 5f\n\t" \
    "buffer_store_dword %[vtag], %[vfo], %[srd], 0 offen sc1\n\t" \
    "5:\n\t" \
    "s_add_u32 m0, %[sfar], %[ldsw]\n\t" \
    "v_mfma_f32_32x32x16_bf16 v[144:159], v[200:203], %[qy0], v[176:191]\n\t" \
    "ds_read_b128 v[200:203], %[nxt] offset:0\n\t" \
    "buffer_load_dwordx4 %[vlane], %[srd], %[soff] offen offset:0 sc1 lds\n\t" \
    "buffer_load_dwordx4 %[vlane], %[srd], %[soff] offen offset:1024 sc1 lds\n\t" \
    "v_exp_f32 v134, v134\n\t" \
    "v_exp_f32 v135, v135\n\t" \
    "v_cvt_pk_bf16_f32 v192, v128, v129\n\t" \
    "v_cvt_pk_bf16_f32 v193, v130, v131\n\t" \
    "v_mfma_f32_32x32x16_bf16 v[144:159], v[204:207], %[qy1], v[144:159]\n\t" \
    "ds_read_b128 v[204:207], %[nxt] offset:1024\n\t" \
    "v_cvt_pk_bf16_f32 v194, v132, v133\n\t" \
    "v_cvt_pk_bf16_f32 v195, v134, v135\n\t" \
    "v_exp_f32 v136, v136\n\t" \
    "v_exp_f32 v137, v137\n\t" \
    "s_waitcnt lgkmcnt(3)\n\t" \
    "v_mfma_f32_32x32x16_bf16 %[OX], v[208:211], v[192:195], %[OX]\n\t" \
    "buffer_load_dwordx4 %[vlane], %[srd], %[soff] offen offset:2048 sc1 lds\n\t" \
    "buffer_load_dwordx4 %[vlane], %[srd], %[soff] offen offset:3072 sc1 lds\n\t" \
    "s_add_u32 m0, m0, 0x4000\n\t" \
    "s_add_u32 %[tmp], %[soff], 0x20000\n\t" \
    "v_exp_f32 v138, v138\n\t" \
    "v_exp_f32 v139, v139\n\t" \
    "v_exp_f32 v140, v140\n\t" \
    "v_mfma_f32_16x16x32_bf16 %[LX], %[ones], v[192:195], %[LX]\n\t" \
    "v_exp_f32 v141, v141\n\t" \
    "v_exp_f32 v142, v142\n\t" \
    "v_exp_f32 v143, v143\n\t" \
    "v_cvt_pk_bf16_f32 v196, v136, v137\n\t" \
    "v_cvt_pk_bf16_f32 v197, v138, v139\n\t" \
    "v_cvt_pk_bf16_f32 v198, v140, v141\n\t" \
    "v_cvt_pk_bf16_f32 v199, v142, v143\n\t" \
    "v_exp_f32 v144, v144\n\t" \
    "v_exp_f32 v145, v145\n\t" \
    "s_waitcnt lgkmcnt(0)\n\t" \
    "v_mfma_f32_32x32x16_bf16 %[OX], v[212:215], v[196:199], %[OX]\n\t" \
    "v_exp_f32 v146, v146\n\t" \
    "v_exp_f32 v147, v147\n\t" \
    "v_exp_f32 v148, v148\n\t" \
    "v_mfma_f32_16x16x32_bf16 %[LX], %[ones], v[196:199], %[LX]\n\t" \
    "v_exp_f32 v149, v149\n\t" \
    "v_mfma_f32_32x32x16_bf16 v[128:143], v[200:203], %[qx0], v[160:175]\n\t" \
    "buffer_load_dwordx4 %[vlane], %[srd], %[tmp] offen offset:0 sc1 lds\n\t" \
    "buffer_load_dwordx4 %[vlane], %[srd], %[tmp] offen offset:1024 sc1 lds\n\t" \
    "v_exp_f32 v150, v150\n\t" \
    "v_exp_f32 v151, v151\n\t" \
    "v_cvt_pk_bf16_f32 v192, v144, v145\n\t" \
    "v_cvt_pk_bf16_f32 v193, v146, v147\n\t" \
    "v_mfma_f32_32x32x16_bf16 v[128:143], v[204:207], %[qx1], v[128:143]\n\t" \
    "v_cvt_pk_bf16_f32 v194, v148, v149\n\t" \
    "v_cvt_pk_bf16_f32 v195, v150, v151\n\t" \
    "v_exp_f32 v152, v152\n\t" \
    "v_exp_f32 v153, v153\n\t" \
    "v_mfma_f32_32x32x16_bf16 %[OY], v[208:211], v[192:195], %[OY]\n\t" \
    "ds_read_b128 v[208:211], %[nxt] offset:2048\n\t" \
    "buffer_load_dwordx4 %[vlane], %[srd], %[tmp] offen offset:2048 sc1 lds\n\t" \
    "buffer_load_dwordx4 %[vlane], %[srd], %[tmp] offen offset:3072 sc1 lds\n\t" \
    "s_add_u32 %[soff], %[soff], 0x4000\n\t" \
    "s_mov_b32 m0, %[pm0]\n\t" \
    "s_nop 0\n\t" \
    "buffer_load_dwordx4 %[vlane], %[psrd], %[psoff] offen lds\n\t" \
    "s_add_u32 %[psoff], %[psoff], 0x800\n\t" \
    "buffer_load_dwordx4 %[vlane], %[psrd], %[psoff] offen lds\n\t" \
    "s_add_u32 %[psoff], %[psoff], 0x800\n\t" \
    "v_exp_f32 v154, v154\n\t" \
    "v_exp_f32 v155, v155\n\t" \
    "v_exp_f32 v156, v156\n\t" \
    "v_mfma_f32_16x16x32_bf16 %[LY], %[ones], v[192:195], %[LY]\n\t" \
    "v_exp_f32 v157, v157\n\t" \
    "v_exp_f32 v158, v158\n\t" \
    "v_exp_f32 v159, v159\n\t" \
    "v_cvt_pk_bf16_f32 v196, v152, v153\n\t" \
    "v_cvt_pk_bf16_f32 v197, v154, v155\n\t" \
    "v_cvt_pk_bf16_f32 v198, v156, v157\n\t" \
    "v_cvt_pk_bf16_f32 v199, v158, v159\n\t" \
    "v_exp_f32 v128, v128\n\t" \
    "v_exp_f32 v129, v129\n\t" \
    "v_mov_b32 %[cur], %[nxt]\n\t" \
    "v_add_u32 %[nxt], %[sfar], %[vgrp]\n\t" \
    "s_add_u32 %[tmp], %[sfar], 0x8000\n\t" \
    "s_cmp_eq_u32 %[sfar], 0x20000\n\t" \
    "s_cselect_b32 %[sfar], 0x10000, %[tmp]\n\t" \
    "s_mov_b32 %[pm0], %[pm1]\n\t" \
    "s_lshr_b32 %[pm1], %[ldsw], 2\n\t" \
    "s_add_u32 %[pm1], %[pm1], 0xe000\n\t" \
    "s_add_u32 %[cnt], %[cnt], 1\n\t" \
    "s_cmp_lt_u32 %[cnt], 6\n\t" \
    "s_cbranch_scc1 1b\n\t" \
    "v_mfma_f32_32x32x16_bf16 %[OY], v[212:215], v[196:199], %[OY]\n\t" \
    "ds_read_b128 v[212:215], %[cur] offset:3072\n\t" \
    "v_exp_f32 v130, v130\n\t" \
    "v_exp_f32 v131, v131\n\t" \
    "v_exp_f32 v132, v132\n\t" \
    "v_mfma_f32_16x16x32_bf16 %[LY], %[ones], v[196:199], %[LY]\n\t" \
    "v_exp_f32 v133, v133\n\t" \
    "v_mfma_f32_32x32x16_bf16 v[144:159], v[200:203], %[qy0], v[176:191]\n\t" \
    "ds_read_b128 v[200:203], %[cur] offset:4096\n\t" \
    "s_mov_b32 m0, %[pm0]\n\t" \
    "s_nop 0\n\t" \
    "buffer_load_dwordx4 %[vlane], %[psrd], %[psoff] offen lds\n\t" \
    "s_add_u32 %[psoff], %[psoff], 0x800\n\t" \
    "buffer_load_dwordx4 %[vlane], %[psrd], %[psoff] offen lds\n\t" \
    "s_add_u32 %[psoff], %[psoff], 0x800\n\t" \
    "v_exp_f32 v134, v134\n\t" \
    "v_exp_f32 v135, v135\n\t" \
    "v_cvt_pk_bf16_f32 v192, v128, v129\n\t" \
    "v_cvt_pk_bf16_f32 v193, v130, v131\n\t" \
    "v_mfma_f32_32x32x16_bf16 v[144:159], v[204:207], %[qy1], v[144:159]\n\t" \
    "ds_read_b128 v[204:207], %[cur] offset:5120\n\t" \
    "v_cvt_pk_bf16_f32 v194, v132, v133\n\t" \
    "v_cvt_pk_bf16_f32 v195, v134, v135\n\t" \
    "v_exp_f32 v136, v136\n\t" \
    "v_exp_f32 v137, v137\n\t" \
    "s_waitcnt lgkmcnt(3)\n\t" \
    "v_mfma_f32_32x32x16_bf16 %[OX], v[208:211], v[192:195], %[OX]\n\t" \
    "v_exp_f32 v138, v138\n\t" \
    "v_exp_f32 v139, v139\n\t" \
    "v_exp_f32 v140, v140\n\t" \
    "v_mfma_f32_16x16x32_bf16 %[LX], %[ones], v[192:195], %[LX]\n\t" \
    "v_exp_f32 v141, v141\n\t" \
    "v_exp_f32 v142, v142\n\t" \
    "v_exp_f32 v143, v143\n\t" \
    "v_cvt_pk_bf16_f32 v196, v136, v137\n\t" \
    "v_cvt_pk_bf16_f32 v197, v138, v139\n\t" \
    "v_cvt_pk_bf16_f32 v198, v140, v141\n\t" \
    "v_cvt_pk_bf16_f32 v199, v142, v143\n\t" \
    "v_exp_f32 v144, v144\n\t" \
    "v_exp_f32 v145, v145\n\t" \
    "s_waitcnt lgkmcnt(0)\n\t" \
    "v_mfma_f32_32x32x16_bf16 %[OX], v[212:215], v[196:199], %[OX]\n\t" \
    "v_exp_f32 v146, v146\n\t" \
    "v_exp_f32 v147, v147\n\t" \
    "v_exp_f32 v148, v148\n\t" \
    "v_mfma_f32_16x16x32_bf16 %[LX], %[ones], v[196:199], %[LX]\n\t" \
    "v_exp_f32 v149, v149\n\t" \
    "v_mfma_f32_32x32x16_bf16 v[128:143], v[200:203], %[qx0], v[160:175]\n\t" \
    "v_exp_f32 v150, v150\n\t" \
    "v_exp_f32 v151, v151\n\t" \
    "v_cvt_pk_bf16_f32 v192, v144, v145\n\t" \
    "v_cvt_pk_bf16_f32 v193, v146, v147\n\t" \
    "v_mfma_f32_32x32x16_bf16 v[128:143], v[204:207], %[qx1], v[128:143]\n\t" \
    "v_cvt_pk_bf16_f32 v194, v148, v149\n\t" \
    "v_cvt_pk_bf16_f32 v195, v150, v151\n\t" \
    "v_exp_f32 v152, v152\n\t" \
    "v_exp_f32 v153, v153\n\t" \
    "v_mfma_f32_32x32x16_bf16 %[OY], v[208:211], v[192:195], %[OY]\n\t" \
    "ds_read_b128 v[208:211], %[cur] offset:6144\n\t" \
    "v_exp_f32 v154, v154\n\t" \
    "v_exp_f32 v155, v155\n\t" \
    "v_exp_f32 v156, v156\n\t" \
    "v_mfma_f32_16x16x32_bf16 %[LY], %[ones], v[192:195], %[LY]\n\t" \
    "v_exp_f32 v157, v157\n\t" \
    "v_exp_f32 v158, v158\n\t" \
    "v_exp_f32 v159, v159\n\t" \
    "v_cvt_pk_bf16_f32 v196, v152, v153\n\t" \
    "v_cvt_pk_bf16_f32 v197, v154, v155\n\t" \
    "v_cvt_pk_bf16_f32 v198, v156, v157\n\t" \
    "v_cvt_pk_bf16_f32 v199, v158, v159\n\t" \
    "v_exp_f32 v128, v128\n\t" \
    "v_exp_f32 v129, v129\n\t" \
    "v_mfma_f32_32x32x16_bf16 %[OY], v[212:215], v[196:199], %[OY]\n\t" \
    "ds_read_b128 v[212:215], %[cur] offset:7168\n\t" \
    "v_exp_f32 v130, v130\n\t" \
    "v_exp_f32 v131, v131\n\t" \
    "v_exp_f32 v132, v132\n\t" \
    "v_mfma_f32_16x16x32_bf16 %[LY], %[ones], v[196:199], %[LY]\n\t" \
    "v_exp_f32 v133, v133\n\t" \
    "v_mfma_f32_32x32x16_bf16 v[144:159], v[200:203], %[qy0], v[176:191]\n\t" \
    "ds_read_b128 v[200:203], %[cur] offset:8192\n\t" \
    "s_mov_b32 m0, %[pm0]\n\t" \
    "s_nop 0\n\t" \
    "buffer_load_dwordx4 %[vlane], %[psrd], %[psoff] offen lds\n\t" \
    "s_add_u32 %[psoff], %[psoff], 0x800\n\t" \
    "buffer_load_dwordx4 %[vlane], %[psrd], %[psoff] offen lds\n\t" \
    "s_add_u32 %[psoff], %[psoff], 0x800\n\t" \
    "v_exp_f32 v134, v134\n\t" \
    "v_exp_f32 v135, v135\n\t" \
    "v_cvt_pk_bf16_f32 v192, v128, v129\n\t" \
    "v_cvt_pk_bf16_f32 v193, v130, v131\n\t" \
    "v_mfma_f32_32x32x16_bf16 v[144:159], v[204:207], %[qy1], v[144:159]\n\t" \
    "ds_read_b128 v[204:207], %[cur] offset:9216\n\t" \
    "v_cvt_pk_bf16_f32 v194, v132, v133\n\t" \
    "v_cvt_pk_bf16_f32 v195, v134, v135\n\t" \
    "v_exp_f32 v136, v136\n\t" \
    "v_exp_f32 v137, v137\n\t" \
    "s_waitcnt lgkmcnt(3)\n\t" \
    "v_mfma_f32_32x32x16_bf16 %[OX], v[208:211], v[192:195], %[OX]\n\t" \
    "v_exp_f32 v138, v138\n\t" \
    "v_exp_f32 v139, v139\n\t" \
    "v_exp_f32 v140, v140\n\t" \
    "v_mfma_f32_16x16x32_bf16 %[LX], %[ones], v[192:195], %[LX]\n\t" \
    "v_exp_f32 v141, v141\n\t" \
    "v_exp_f32 v142, v142\n\t" \
    "v_exp_f32 v143, v143\n\t" \
    "v_cvt_pk_bf16_f32 v196, v136, v137\n\t" \
    "v_cvt_pk_bf16_f32 v197, v138, v139\n\t" \
    "v_cvt_pk_bf16_f32 v198, v140, v141\n\t" \
    "v_cvt_pk_bf16_f32 v199, v142, v143\n\t" \
    "v_exp_f32 v144, v144\n\t" \
    "v_exp_f32 v145, v145\n\t" \
    "s_waitcnt lgkmcnt(0)\n\t" \
    "v_mfma_f32_32x32x16_bf16 %[OX], v[212:215], v[196:199], %[OX]\n\t" \
    "v_exp_f32 v146, v146\n\t" \
    "v_exp_f32 v147, v147\n\t" \
    "v_exp_f32 v148, v148\n\t" \
    "v_mfma_f32_16x16x32_bf16 %[LX], %[ones], v[196:199], %[LX]\n\t" \
    "v_exp_f32 v149, v149\n\t" \
    "v_mfma_f32_32x32x16_bf16 v[128:143], v[200:203], %[qx0], v[160:175]\n\t" \
    "v_exp_f32 v150, v150\n\t" \
    "v_exp_f32 v151, v151\n\t" \
    "v_cvt_pk_bf16_f32 v192, v144, v145\n\t" \
    "v_cvt_pk_bf16_f32 v193, v146, v147\n\t" \
    "v_mfma_f32_32x32x16_bf16 v[128:143], v[204:207], %[qx1], v[128:143]\n\t" \
    "v_cvt_pk_bf16_f32 v194, v148, v149\n\t" \
    "v_cvt_pk_bf16_f32 v195, v150, v151\n\t" \
    "v_exp_f32 v152, v152\n\t" \
    "v_exp_f32 v153, v153\n\t" \
    "v_mfma_f32_32x32x16_bf16 %[OY], v[208:211], v[192:195], %[OY]\n\t" \
    "ds_read_b128 v[208:211], %[cur] offset:10240\n\t" \
    "v_exp_f32 v154, v154\n\t" \
    "v_exp_f32 v155, v155\n\t" \
    "v_exp_f32 v156, v156\n\t" \
    "v_mfma_f32_16x16x32_bf16 %[LY], %[ones], v[192:195], %[LY]\n\t" \
    "v_exp_f32 v157, v157\n\t" \
    "v_exp_f32 v158, v158\n\t" \
    "v_exp_f32 v159, v159\n\t" \
    "v_cvt_pk_bf16_f32 v196, v152, v153\n\t" \
    "v_cvt_pk_bf16_f32 v197, v154, v155\n\t" \
    "v_cvt_pk_bf16_f32 v198, v156, v157\n\t" \
    "v_cvt_pk_bf16_f32 v199, v158, v159\n\t" \
    "v_exp_f32 v128, v128\n\t" \
    "v_exp_f32 v129, v129\n\t" \
    "v_mfma_f32_32x32x16_bf16 %[OY], v[212:215], v[196:199], %[OY]\n\t" \
    "ds_read_b128 v[212:215], %[cur] offset:11264\n\t" \
    "v_exp_f32 v130, v130\n\t" \
    "v_exp_f32 v131, v131\n\t" \
    "v_exp_f32 v132, v132\n\t" \
    "v_mfma_f32_16x16x32_bf16 %[LY], %[ones], v[196:199], %[LY]\n\t" \
    "v_exp_f32 v133, v133\n\t" \
    "v_mfma_f32_32x32x16_bf16 v[144:159], v[200:203], %[qy0], v[176:191]\n\t" \
    "ds_read_b128 v[200:203], %[cur] offset:12288\n\t" \
    "v_exp_f32 v134, v134\n\t" \
    "v_exp_f32 v135, v135\n\t" \
    "v_cvt_pk_bf16_f32 v192, v128, v129\n\t" \
    "v_cvt_pk_bf16_f32 v193, v130, v131\n\t" \
    "v_mfma_f32_32x32x16_bf16 v[144:159], v[204:207], %[qy1], v[144:159]\n\t" \
    "ds_read_b128 v[204:207], %[cur] offset:13312\n\t" \
    "v_cvt_pk_bf16_f32 v194, v132, v133\n\t" \
    "v_cvt_pk_bf16_f32 v195, v134, v135\n\t" \
    "v_exp_f32 v136, v136\n\t" \
    "v_exp_f32 v137, v137\n\t" \
    "s_waitcnt lgkmcnt(3)\n\t" \
    "v_mfma_f32_32x32x16_bf16 %[OX], v[208:211], v[192:195], %[OX]\n\t" \
    "v_exp_f32 v138, v138\n\t" \
    "v_exp_f32 v139, v139\n\t" \
    "v_exp_f32 v140, v140\n\t" \
    "v_mfma_f32_16x16x32_bf16 %[LX], %[ones], v[192:195], %[LX]\n\t" \
    "v_exp_f32 v141, v141\n\t" \
    "v_exp_f32 v142, v142\n\t" \
    "v_exp_f32 v143, v143\n\t" \
    "v_cvt_pk_bf16_f32 v196, v136, v137\n\t" \
    "v_cvt_pk_bf16_f32 v197, v138, v139\n\t" \
    "v_cvt_pk_bf16_f32 v198, v140, v141\n\t" \
    "v_cvt_pk_bf16_f32 v199, v142, v143\n\t" \
    "v_exp_f32 v144, v144\n\t" \
    "v_exp_f32 v145, v145\n\t" \
    "s_waitcnt lgkmcnt(0)\n\t" \
    "v_mfma_f32_32x32x16_bf16 %[OX], v[212:215], v[196:199], %[OX]\n\t" \
    "v_exp_f32 v146, v146\n\t" \
    "v_exp_f32 v147, v147\n\t" \
    "v_exp_f32 v148, v148\n\t" \
    "v_mfma_f32_16x16x32_bf16 %[LX], %[ones], v[196:199], %[LX]\n\t" \
    "v_exp_f32 v149, v149\n\t" \
    "v_mfma_f32_32x32x16_bf16 v[128:143], v[200:203], %[qx0], v[160:175]\n\t" \
    "v_exp_f32 v150, v150\n\t" \
    "v_exp_f32 v151, v151\n\t" \
    "v_cvt_pk_bf16_f32 v192, v144, v145\n\t" \
    "v_cvt_pk_bf16_f32 v193, v146, v147\n\t" \
    "v_mfma_f32_32x32x16_bf16 v[128:143], v[204:207], %[qx1], v[128:143]\n\t" \
    "v_cvt_pk_bf16_f32 v194, v148, v149\n\t" \
    "v_cvt_pk_bf16_f32 v195, v150, v151\n\t" \
    "v_exp_f32 v152, v152\n\t" \
    "v_exp_f32 v153, v153\n\t" \
    "v_mfma_f32_32x32x16_bf16 %[OY], v[208:211], v[192:195], %[OY]\n\t" \
    "ds_read_b128 v[208:211], %[cur] offset:14336\n\t" \
    "v_exp_f32 v154, v154\n\t" \
    "v_exp_f32 v155, v155\n\t" \
    "v_exp_f32 v156, v156\n\t" \
    "v_mfma_f32_16x16x32_bf16 %[LY], %[ones], v[192:195], %[LY]\n\t" \
    "v_exp_f32 v157, v157\n\t" \
    "v_exp_f32 v158, v158\n\t" \
    "v_exp_f32 v159, v159\n\t" \
    "v_cvt_pk_bf16_f32 v196, v152, v153\n\t" \
    "v_cvt_pk_bf16_f32 v197, v154, v155\n\t" \
    "v_cvt_pk_bf16_f32 v198, v156, v157\n\t" \
    "v_cvt_pk_bf16_f32 v199, v158, v159\n\t" \
    "v_exp_f32 v128, v128\n\t" \
    "v_exp_f32 v129, v129\n\t" \
    "v_mfma_f32_32x32x16_bf16 %[OY], v[212:215], v[196:199], %[OY]\n\t" \
    "ds_read_b128 v[212:215], %[cur] offset:15360\n\t" \
    "v_exp_f32 v130, v130\n\t" \
    "v_exp_f32 v131, v131\n\t" \
    "v_exp_f32 v132, v132\n\t" \
    "v_mfma_f32_16x16x32_bf16 %[LY], %[ones], v[196:199], %[LY]\n\t" \
    "v_exp_f32 v133, v133\n\t" \
    "s_waitcnt vmcnt(6)\n\t" \
    "s_barrier\n\t" \
    "v_mfma_f32_32x32x16_bf16 v[144:159], v[200:203], %[qy0], v[176:191]\n\t" \
    "ds_read_b128 v[200:203], %[nxt] offset:0\n\t" \
    "v_exp_f32 v134, v134\n\t" \
    "v_exp_f32 v135, v135\n\t" \
    "v_cvt_pk_bf16_f32 v192, v128, v129\n\t" \
    "v_cvt_pk_bf16_f32 v193, v130, v131\n\t" \
    "v_mfma_f32_32x32x16_bf16 v[144:159], v[204:207], %[qy1], v[144:159]\n\t" \
    "ds_read_b128 v[204:207], %[nxt] offset:1024\n\t" \
    "v_cvt_pk_bf16_f32 v194, v132, v133\n\t" \
    "v_cvt_pk_bf16_f32 v195, v134, v135\n\t" \
    "v_exp_f32 v136, v136\n\t" \
    "v_exp_f32 v137, v137\n\t" \
    "s_waitcnt lgkmcnt(3)\n\t" \
    "v_mfma_f32_32x32x16_bf16 %[OX], v[208:211], v[192:195], %[OX]\n\t" \
    "v_exp_f32 v138, v138\n\t" \
    "v_exp_f32 v139, v139\n\t" \
    "v_exp_f32 v140, v140\n\t" \
    "v_mfma_f32_16x16x32_bf16 %[LX], %[ones], v[192:195], %[LX]\n\t" \
    "v_exp_f32 v141, v141\n\t" \
    "v_exp_f32 v142, v142\n\t" \
    "v_exp_f32 v143, v143\n\t" \
    "v_cvt_pk_bf16_f32 v196, v136, v137\n\t" \
    "v_cvt_pk_bf16_f32 v197, v138, v139\n\t" \
    "v_cvt_pk_bf16_f32 v198, v140, v141\n\t" \
    "v_cvt_pk_bf16_f32 v199, v142, v143\n\t" \
    "v_exp_f32 v144, v144\n\t" \
    "v_exp_f32 v145, v145\n\t" \
    "s_waitcnt lgkmcnt(0)\n\t" \
    "v_mfma_f32_32x32x16_bf16 %[OX], v[212:215], v[196:199], %[OX]\n\t" \
    "v_exp_f32 v146, v146\n\t" \
    "v_exp_f32 v147, v147\n\t" \
    "v_exp_f32 v148, v148\n\t" \
    "v_mfma_f32_16x16x32_bf16 %[LX], %[ones], v[196:199], %[LX]\n\t" \
    "v_exp_f32 v149, v149\n\t" \
    "v_mfma_f32_32x32x16_bf16 v[128:143], v[200:203], %[qx0], v[160:175]\n\t" \
    "v_exp_f32 v150, v150\n\t" \
    "v_exp_f32 v151, v151\n\t" \
    "v_cvt_pk_bf16_f32 v192, v144, v145\n\t" \
    "v_cvt_pk_bf16_f32 v193, v146, v147\n\t" \
    "v_mfma_f32_32x32x16_bf16 v[128:143], v[204:207], %[qx1], v[128:143]\n\t" \
    "v_cvt_pk_bf16_f32 v194, v148, v149\n\t" \
    "v_cvt_pk_bf16_f32 v195, v150, v151\n\t" \
    "v_exp_f32 v152, v152\n\t" \
    "v_exp_f32 v153, v153\n\t" \
    "v_mfma_f32_32x32x16_bf16 %[OY], v[208:211], v[192:195], %[OY]\n\t" \
    "ds_read_b128 v[208:211], %[nxt] offset:2048\n\t" \
    "v_exp_f32 v154, v154\n\t" \
    "v_exp_f32 v155, v155\n\t" \
    "v_exp_f32 v156, v156\n\t" \
    "v_mfma_f32_16x16x32_bf16 %[LY], %[ones], v[192:195], %[LY]\n\t" \
    "v_exp_f32 v157, v157\n\t" \
    "v_exp_f32 v158, v158\n\t" \
    "v_exp_f32 v159, v159\n\t" \
    "v_cvt_pk_bf16_f32 v196, v152, v153\n\t" \
    "v_cvt_pk_bf16_f32 v197, v154, v155\n\t" \
    "v_cvt_pk_bf16_f32 v198, v156, v157\n\t" \
    "v_cvt_pk_bf16_f32 v199, v158, v159\n\t" \
    "v_exp_f32 v128, v128\n\t" \
    "v_exp_f32 v129, v129\n\t" \
    "v_mov_b32 %[cur], %[nxt]\n\t" \
    "v_add_u32 %[nxt], %[sfar], %[vgrp]\n\t" \
    "v_mfma_f32_32x32x16_bf16 %[OY], v[212:215], v[196:199], %[OY]\n\t" \
    "ds_read_b128 v[212:215], %[cur] offset:3072\n\t" \
    "v_exp_f32 v130, v130\n\t" \
    "v_exp_f32 v131, v131\n\t" \
    "v_exp_f32 v132, v132\n\t" \
    "v_mfma_f32_16x16x32_bf16 %[LY], %[ones], v[196:199], %[LY]\n\t" \
    "v_exp_f32 v133, v133\n\t" \
    "v_mfma_f32_32x32x16_bf16 v[144:159], v[200:203], %[qy0], v[176:191]\n\t" \
    "ds_read_b128 v[200:203], %[cur] offset:4096\n\t" \
    "v_exp_f32 v134, v134\n\t" \
    "v_exp_f32 v135, v135\n\t" \
    "v_cvt_pk_bf16_f32 v192, v128, v129\n\t" \
    "v_cvt_pk_bf16_f32 v193, v130, v131\n\t" \
    "v_mfma_f32_32x32x16_bf16 v[144:159], v[204:207], %[qy1], v[144:159]\n\t" \
    "ds_read_b128 v[204:207], %[cur] offset:5120\n\t" \
    "v_cvt_pk_bf16_f32 v194, v132, v133\n\t" \
    "v_cvt_pk_bf16_f32 v195, v134, v135\n\t" \
    "v_exp_f32 v136, v136\n\t" \
    "v_exp_f32 v137, v137\n\t" \
    "s_waitcnt lgkmcnt(3)\n\t" \
    "v_mfma_f32_32x32x16_bf16 %[OX], v[208:211], v[192:195], %[OX]\n\t" \
    "v_exp_f32 v138, v138\n\t" \
    "v_exp_f32 v139, v139\n\t" \
    "v_exp_f32 v140, v140\n\t" \
    "v_mfma_f32_16x16x32_bf16 %[LX], %[ones], v[192:195], %[LX]\n\t" \
    "v_exp_f32 v141, v141\n\t" \
    "v_exp_f32 v142, v142\n\t" \
    "v_exp_f32 v143, v143\n\t" \
    "v_cvt_pk_bf16_f32 v196, v136, v137\n\t" \
    "v_cvt_pk_bf16_f32 v197, v138, v139\n\t" \
    "v_cvt_pk_bf16_f32 v198, v140, v141\n\t" \
    "v_cvt_pk_bf16_f32 v199, v142, v143\n\t" \
    "v_exp_f32 v144, v144\n\t" \
    "v_exp_f32 v145, v145\n\t" \
    "s_waitcnt lgkmcnt(0)\n\t" \
    "v_mfma_f32_32x32x16_bf16 %[OX], v[212:215], v[196:199], %[OX]\n\t" \
    "v_exp_f32 v146, v146\n\t" \
    "v_exp_f32 v147, v147\n\t" \
    "v_exp_f32 v148, v148\n\t" \
    "v_mfma_f32_16x16x32_bf16 %[LX], %[ones], v[196:199], %[LX]\n\t" \
    "v_exp_f32 v149, v149\n\t" \
    "v_mfma_f32_32x32x16_bf16 v[128:143], v[200:203], %[qx0], v[160:175]\n\t" \
    "v_exp_f32 v150, v150\n\t" \
    "v_exp_f32 v151, v151\n\t" \
    "v_cvt_pk_bf16_f32 v192, v144, v145\n\t" \
    "v_cvt_pk_bf16_f32 v193, v146, v147\n\t" \
    "v_mfma_f32_32x32x16_bf16 v[128:143], v[204:207], %[qx1], v[128:143]\n\t" \
    "v_cvt_pk_bf16_f32 v194, v148, v149\n\t" \
    "v_cvt_pk_bf16_f32 v195, v150, v151\n\t" \
    "v_exp_f32 v152, v152\n\t" \
    "v_exp_f32 v153, v153\n\t" \
    "v_mfma_f32_32x32x16_bf16 %[OY], v[208:211], v[192:195], %[OY]\n\t" \
    "ds_read_b128 v[208:211], %[cur] offset:6144\n\t" \
    "v_exp_f32 v154, v154\n\t" \
    "v_exp_f32 v155, v155\n\t" \
    "v_exp_f32 v156, v156\n\t" \
    "v_mfma_f32_16x16x32_bf16 %[LY], %[ones], v[192:195], %[LY]\n\t" \
    "v_exp_f32 v157, v157\n\t" \
    "v_exp_f32 v158, v158\n\t" \
    "v_exp_f32 v159, v159\n\t" \
    "v_cvt_pk_bf16_f32 v196, v152, v153\n\t" \
    "v_cvt_pk_bf16_f32 v197, v154, v155\n\t" \
    "v_cvt_pk_bf16_f32 v198, v156, v157\n\t" \
    "v_cvt_pk_bf16_f32 v199, v158, v159\n\t" \
    "v_exp_f32 v128, v128\n\t" \
    "v_exp_f32 v129, v129\n\t" \
    "v_mfma_f32_32x32x16_bf16 %[OY], v[212:215], v[196:199], %[OY]\n\t" \
    "ds_read_b128 v[212:215], %[cur] offset:7168\n\t" \
    "v_exp_f32 v130, v130\n\t" \
    "v_exp_f32 v131, v131\n\t" \
    "v_exp_f32 v132, v132\n\t" \
    "v_mfma_f32_16x16x32_bf16 %[LY], %[ones], v[196:199], %[LY]\n\t" \
    "v_exp_f32 v133, v133\n\t" \
    "v_mfma_f32_32x32x16_bf16 v[144:159], v[200:203], %[qy0], v[176:191]\n\t" \
    "ds_read_b128 v[200:203], %[cur] offset:8192\n\t" \
    "v_exp_f32 v134, v134\n\t" \
    "v_exp_f32 v135, v135\n\t" \
    "v_cvt_pk_bf16_f32 v192, v128, v129\n\t" \
    "v_cvt_pk_bf16_f32 v193, v130, v131\n\t" \
    "v_mfma_f32_32x32x16_bf16 v[144:159], v[204:207], %[qy1], v[144:159]\n\t" \
    "ds_read_b128 v[204:207], %[cur] offset:9216\n\t" \
    "v_cvt_pk_bf16_f32 v194, v132, v133\n\t" \
    "v_cvt_pk_bf16_f32 v195, v134, v135\n\t" \
    "v_exp_f32 v136, v136\n\t" \
    "v_exp_f32 v137, v137\n\t" \
    "s_waitcnt lgkmcnt(3)\n\t" \
    "v_mfma_f32_32x32x16_bf16 %[OX], v[208:211], v[192:195], %[OX]\n\t" \
    "v_exp_f32 v138, v138\n\t" \
    "v_exp_f32 v139, v139\n\t" \
    "v_exp_f32 v140, v140\n\t" \
    "v_mfma_f32_16x16x32_bf16 %[LX], %[ones], v[192:195], %[LX]\n\t" \
    "v_exp_f32 v141, v141\n\t" \
    "v_exp_f32 v142, v142\n\t" \
    "v_exp_f32 v143, v143\n\t" \
    "v_cvt_pk_bf16_f32 v196, v136, v137\n\t" \
    "v_cvt_pk_bf16_f32 v197, v138, v139\n\t" \
    "v_cvt_pk_bf16_f32 v198, v140, v141\n\t" \
    "v_cvt_pk_bf16_f32 v199, v142, v143\n\t" \
    "v_exp_f32 v144, v144\n\t" \
    "v_exp_f32 v145, v145\n\t" \
    "s_waitcnt lgkmcnt(0)\n\t" \
    "v_mfma_f32_32x32x16_bf16 %[OX], v[212:215], v[196:199], %[OX]\n\t" \
    "v_exp_f32 v146, v146\n\t" \
    "v_exp_f32 v147, v147\n\t" \
    "v_exp_f32 v148, v148\n\t" \
    "v_mfma_f32_16x16x32_bf16 %[LX], %[ones], v[196:199], %[LX]\n\t" \
    "v_exp_f32 v149, v149\n\t" \
    "v_mfma_f32_32x32x16_bf16 v[128:143], v[200:203], %[qx0], v[160:175]\n\t" \
    "v_exp_f32 v150, v150\n\t" \
    "v_exp_f32 v151, v151\n\t" \
    "v_cvt_pk_bf16_f32 v192, v144, v145\n\t" \
    "v_cvt_pk_bf16_f32 v193, v146, v147\n\t" \
    "v_mfma_f32_32x32x16_bf16 v[128:143], v[204:207], %[qx1], v[128:143]\n\t" \
    "v_cvt_pk_bf16_f32 v194, v148, v149\n\t" \
    "v_cvt_pk_bf16_f32 v195, v150, v151\n\t" \
    "v_exp_f32 v152, v152\n\t" \
    "v_exp_f32 v153, v153\n\t" \
    "v_mfma_f32_32x32x16_bf16 %[OY], v[208:211], v[192:195], %[OY]\n\t" \
    "ds_read_b128 v[208:211], %[cur] offset:10240\n\t" \
    "v_exp_f32 v154, v154\n\t" \
    "v_exp_f32 v155, v155\n\t" \
    "v_exp_f32 v156, v156\n\t" \
    "v_mfma_f32_16x16x32_bf16 %[LY], %[ones], v[192:195], %[LY]\n\t" \
    "v_exp_f32 v157, v157\n\t" \
    "v_exp_f32 v158, v158\n\t" \
    "v_exp_f32 v159, v159\n\t" \
    "v_cvt_pk_bf16_f32 v196, v152, v153\n\t" \
    "v_cvt_pk_bf16_f32 v197, v154, v155\n\t" \
    "v_cvt_pk_bf16_f32 v198, v156, v157\n\t" \
    "v_cvt_pk_bf16_f32 v199, v158, v159\n\t" \
    "v_exp_f32 v128, v128\n\t" \
    "v_exp_f32 v129, v129\n\t" \
    "v_mfma_f32_32x32x16_bf16 %[OY], v[212:215], v[196:199], %[OY]\n\t" \
    "ds_read_b128 v[212:215], %[cur] offset:11264\n\t" \
    "v_exp_f32 v130, v130\n\t" \
    "v_exp_f32 v131, v131\n\t" \
    "v_exp_f32 v132, v132\n\t" \
    "v_mfma_f32_16x16x32_bf16 %[LY], %[ones], v[196:199], %[LY]\n\t" \
    "v_exp_f32 v133, v133\n\t" \
    "v_mfma_f32_32x32x16_bf16 v[144:159], v[200:203], %[qy0], v[176:191]\n\t" \
    "ds_read_b128 v[200:203], %[cur] offset:12288\n\t" \
    "v_exp_f32 v134, v134\n\t" \
    "v_exp_f32 v135, v135\n\t" \
    "v_cvt_pk_bf16_f32 v192, v128, v129\n\t" \
    "v_cvt_pk_bf16_f32 v193, v130, v131\n\t" \
    "v_mfma_f32_32x32x16_bf16 v[144:159], v[204:207], %[qy1], v[144:159]\n\t" \
    "ds_read_b128 v[204:207], %[cur] offset:13312\n\t" \
    "v_cvt_pk_bf16_f32 v194, v132, v133\n\t" \
    "v_cvt_pk_bf16_f32 v195, v134, v135\n\t" \
    "v_exp_f32 v136, v136\n\t" \
    "v_exp_f32 v137, v137\n\t" \
    "s_waitcnt lgkmcnt(3)\n\t" \
    "v_mfma_f32_32x32x16_bf16 %[OX], v[208:211], v[192:195], %[OX]\n\t" \
    "v_exp_f32 v138, v138\n\t" \
    "v_exp_f32 v139, v139\n\t" \
    "v_exp_f32 v140, v140\n\t" \
    "v_mfma_f32_16x16x32_bf16 %[LX], %[ones], v[192:195], %[LX]\n\t" \
    "v_exp_f32 v141, v141\n\t" \
    "v_exp_f32 v142, v142\n\t" \
    "v_exp_f32 v143, v143\n\t" \
    "v_cvt_pk_bf16_f32 v196, v136, v137\n\t" \
    "v_cvt_pk_bf16_f32 v197, v138, v139\n\t" \
    "v_cvt_pk_bf16_f32 v198, v140, v141\n\t" \
    "v_cvt_pk_bf16_f32 v199, v142, v143\n\t" \
    "v_exp_f32 v144, v144\n\t" \
    "v_exp_f32 v145, v145\n\t" \
    "s_waitcnt lgkmcnt(0)\n\t" \
    "v_mfma_f32_32x32x16_bf16 %[OX], v[212:215], v[196:199], %[OX]\n\t" \
    "v_exp_f32 v146, v146\n\t" \
    "v_exp_f32 v147, v147\n\t" \
    "v_exp_f32 v148, v148\n\t" \
    "v_mfma_f32_16x16x32_bf16 %[LX], %[ones], v[196:199], %[LX]\n\t" \
    "v_exp_f32 v149, v149\n\t" \
    "v_mfma_f32_32x32x16_bf16 v[128:143], v[200:203], %[qx0], v[160:175]\n\t" \
    "v_exp_f32 v150, v150\n\t" \
    "v_exp_f32 v151, v151\n\t" \
    "v_cvt_pk_bf16_f32 v192, v144, v145\n\t" \
    "v_cvt_pk_bf16_f32 v193, v146, v147\n\t" \
    "v_mfma_f32_32x32x16_bf16 v[128:143], v[204:207], %[qx1], v[128:143]\n\t" \
    "v_cvt_pk_bf16_f32 v194, v148, v149\n\t" \
    "v_cvt_pk_bf16_f32 v195, v150, v151\n\t" \
    "v_exp_f32 v152, v152\n\t" \
    "v_exp_f32 v153, v153\n\t" \
    "v_mfma_f32_32x32x16_bf16 %[OY], v[208:211], v[192:195], %[OY]\n\t" \
    "ds_read_b128 v[208:211], %[cur] offset:14336\n\t" \
    "v_exp_f32 v154, v154\n\t" \
    "v_exp_f32 v155, v155\n\t" \
    "v_exp_f32 v156, v156\n\t" \
    "v_mfma_f32_16x16x32_bf16 %[LY], %[ones], v[192:195], %[LY]\n\t" \
    "v_exp_f32 v157, v157\n\t" \
    "v_exp_f32 v158, v158\n\t" \
    "v_exp_f32 v159, v159\n\t" \
    "v_cvt_pk_bf16_f32 v196, v152, v153\n\t" \
    "v_cvt_pk_bf16_f32 v197, v154, v155\n\t" \
    "v_cvt_pk_bf16_f32 v198, v156, v157\n\t" \
    "v_cvt_pk_bf16_f32 v199, v158, v159\n\t" \
    "v_exp_f32 v128, v128\n\t" \
    "v_exp_f32 v129, v129\n\t" \
    "v_mfma_f32_32x32x16_bf16 %[OY], v[212:215], v[196:199], %[OY]\n\t" \
    "ds_read_b128 v[212:215], %[cur] offset:15360\n\t" \
    "v_exp_f32 v130, v130\n\t" \
    "v_exp_f32 v131, v131\n\t" \
    "v_exp_f32 v132, v132\n\t" \
    "v_mfma_f32_16x16x32_bf16 %[LY], %[ones], v[196:199], %[LY]\n\t" \
    "v_exp_f32 v133, v133\n\t" \
    "s_waitcnt vmcnt(0)\n\t" \
    "s_barrier\n\t" \
    "v_mfma_f32_32x32x16_bf16 v[144:159], v[200:203], %[qy0], v[176:191]\n\t" \
    "ds_read_b128 v[200:203], %[nxt] offset:0\n\t" \
    "v_exp_f32 v134, v134\n\t" \
    "v_exp_f32 v135, v135\n\t" \
    "v_cvt_pk_bf16_f32 v192, v128, v129\n\t" \
    "v_cvt_pk_bf16_f32 v193, v130, v131\n\t" \
    "v_mfma_f32_32x32x16_bf16 v[144:159], v[204:207], %[qy1], v[144:159]\n\t" \
    "ds_read_b128 v[204:207], %[nxt] offset:1024\n\t" \
    "v_cvt_pk_bf16_f32 v194, v132, v133\n\t" \
    "v_cvt_pk_bf16_f32 v195, v134, v135\n\t" \
    "v_exp_f32 v136, v136\n\t" \
    "v_exp_f32 v137, v137\n\t" \
    "s_waitcnt lgkmcnt(3)\n\t" \
    "v_mfma_f32_32x32x16_bf16 %[OX], v[208:211], v[192:195], %[OX]\n\t" \
    "v_exp_f32 v138, v138\n\t" \
    "v_exp_f32 v139, v139\n\t" \
    "v_exp_f32 v140, v140\n\t" \
    "v_mfma_f32_16x16x32_bf16 %[LX], %[ones], v[192:195], %[LX]\n\t" \
    "v_exp_f32 v141, v141\n\t" \
    "v_exp_f32 v142, v142\n\t" \
    "v_exp_f32 v143, v143\n\t" \
    "v_cvt_pk_bf16_f32 v196, v136, v137\n\t" \
    "v_cvt_pk_bf16_f32 v197, v138, v139\n\t" \
    "v_cvt_pk_bf16_f32 v198, v140, v141\n\t" \
    "v_cvt_pk_bf16_f32 v199, v142, v143\n\t" \
    "v_exp_f32 v144, v144\n\t" \
    "v_exp_f32 v145, v145\n\t" \
    "s_waitcnt lgkmcnt(0)\n\t" \
    "v_mfma_f32_32x32x16_bf16 %[OX], v[212:215], v[196:199], %[OX]\n\t" \
    "v_exp_f32 v146, v146\n\t" \
    "v_exp_f32 v147, v147\n\t" \
    "v_exp_f32 v148, v148\n\t" \
    "v_mfma_f32_16x16x32_bf16 %[LX], %[ones], v[196:199], %[LX]\n\t" \
    "v_exp_f32 v149, v149\n\t" \
    "v_mfma_f32_32x32x16_bf16 v[128:143], v[200:203], %[qx0], v[160:175]\n\t" \
    "v_exp_f32 v150, v150\n\t" \
    "v_exp_f32 v151, v151\n\t" \
    "v_cvt_pk_bf16_f32 v192, v144, v145\n\t" \
    "v_cvt_pk_bf16_f32 v193, v146, v147\n\t" \
    "v_mfma_f32_32x32x16_bf16 v[128:143], v[204:207], %[qx1], v[128:143]\n\t" \
    "v_cvt_pk_bf16_f32 v194, v148, v149\n\t" \
    "v_cvt_pk_bf16_f32 v195, v150, v151\n\t" \
    "v_exp_f32 v152, v152\n\t" \
    "v_exp_f32 v153, v153\n\t" \
    "v_mfma_f32_32x32x16_bf16 %[OY], v[208:211], v[192:195], %[OY]\n\t" \
    "ds_read_b128 v[208:211], %[nxt] offset:2048\n\t" \
    "v_exp_f32 v154, v154\n\t" \
    "v_exp_f32 v155, v155\n\t" \
    "v_exp_f32 v156, v156\n\t" \
    "v_mfma_f32_16x16x32_bf16 %[LY], %[ones], v[192:195], %[LY]\n\t" \
    "v_exp_f32 v157, v157\n\t" \
    "v_exp_f32 v158, v158\n\t" \
    "v_exp_f32 v159, v159\n\t" \
    "v_cvt_pk_bf16_f32 v196, v152, v153\n\t" \
    "v_cvt_pk_bf16_f32 v197, v154, v155\n\t" \
    "v_cvt_pk_bf16_f32 v198, v156, v157\n\t" \
    "v_cvt_pk_bf16_f32 v199, v158, v159\n\t" \
    "v_exp_f32 v128, v128\n\t" \
    "v_exp_f32 v129, v129\n\t" \
    "v_mov_b32 %[cur], %[nxt]\n\t" \
    "v_add_u32 %[nxt], %[sfar], %[vgrp]\n\t" \
    "s_waitcnt lgkmcnt(0)\n\t" \
    "v_mfma_f32_32x32x16_bf16 %[OY], v[212:215], v[196:199], %[OY]\n\t" \
    "v_mfma_f32_16x16x32_bf16 %[LY], %[ones], v[196:199], %[LY]\n\t" \
    "s_nop 15\n\t"
#define ATTN_A1_ASM \
    "s_nop 1\n\t" \
    "ds_read_b128 v[200:203], %[k0a] offset:0\n\t" \
    "ds_read_b128 v[204:207], %[k0a] offset:1024\n\t" \
    "v_mov_b32 v196, 0\n\t" \
    "v_mov_b32 v197, 0\n\t" \
    "v_mov_b32 v198, 0\n\t" \
    "v_mov_b32 v199, 0\n\t" \
    "v_mov_b32 v212, 0\n\t" \
    "v_mov_b32 v213, 0\n\t" \
    "v_mov_b32 v214, 0\n\t" \
    "v_mov_b32 v215, 0\n\t" \
    "s_waitcnt lgkmcnt(0)\n\t" \
    "v_mfma_f32_32x32x16_bf16 v[128:143], v[200:203], %[qx0], 0\n\t" \
    "v_mfma_f32_32x32x16_bf16 v[128:143], v[204:207], %[qx1], v[128:143]\n\t" \
    "v_mfma_f32_32x32x16_bf16 v[144:159], v[200:203], %[qy0], 0\n\t" \
    "v_mfma_f32_32x32x16_bf16 v[144:159], v[204:207], %[qy1], v[144:159]\n\t" \
    "ds_read_b128 v[200:203], %[cur] offset:0\n\t" \
    "ds_read_b128 v[204:207], %[cur] offset:1024\n\t" \
    "ds_read_b128 v[208:211], %[cur] offset:2048\n\t" \
    "s_nop 8\n\t" \
    "v_max3_f32 v216, v128, v129, v130\n\t" \
    "v_max3_f32 v217, v131, v132, v133\n\t" \
    "v_max3_f32 v216, v216, v134, v135\n\t" \
    "v_max3_f32 v217, v217, v136, v137\n\t" \
    "v_max3_f32 v216, v216, v138, v139\n\t" \
    "v_max3_f32 v217, v217, v140, v141\n\t" \
    "v_max3_f32 v216, v216, v142, v143\n\t" \
    "v_max_f32 v216, v216, v217\n\t" \
    "v_mov_b32 v217, v216\n\t" \
    "s_nop 1\n\t" \
    "v_permlane32_swap_b32 v216, v217\n\t" \
    "s_nop 1\n\t" \
    "v_max_f32 v216, v216, v217\n\t" \
    "v_sub_f32 v160, 0, v216\n\t" \
    "v_sub_f32 v161, 0, v216\n\t" \
    "v_sub_f32 v162, 0, v216\n\t" \
    "v_sub_f32 v163, 0, v216\n\t" \
    "v_sub_f32 v164, 0, v216\n\t" \
    "v_sub_f32 v165, 0, v216\n\t" \
    "v_sub_f32 v166, 0, v216\n\t" \
    "v_sub_f32 v167, 0, v216\n\t" \
    "v_sub_f32 v168, 0, v216\n\t" \
    "v_sub_f32 v169, 0, v216\n\t" \
    "v_sub_f32 v170, 0, v216\n\t" \
    "v_sub_f32 v171, 0, v216\n\t" \
    "v_sub_f32 v172, 0, v216\n\t" \
    "v_sub_f32 v173, 0, v216\n\t" \
    "v_sub_f32 v174, 0, v216\n\t" \
    "v_sub_f32 v175, 0, v216\n\t" \
    "s_waitcnt lgkmcnt(0)\n\t" \
    "s_nop 0\n\t" \
    "v_mfma_f32_32x32x16_bf16 v[128:143], v[200:203], %[qx0], v[160:175]\n\t" \
    "v_mfma_f32_32x32x16_bf16 v[128:143], v[204:207], %[qx1], v[128:143]\n\t" \
    "v_max3_f32 v216, v144, v145, v146\n\t" \
    "v_max3_f32 v217, v147, v148, v149\n\t" \
    "v_max3_f32 v216, v216, v150, v151\n\t" \
    "v_max3_f32 v217, v217, v152, v153\n\t" \
    "v_max3_f32 v216, v216, v154, v155\n\t" \
    "v_max3_f32 v217, v217, v156, v157\n\t" \
    "v_max3_f32 v216, v216, v158, v159\n\t" \
    "v_max_f32 v216, v216, v217\n\t" \
    "v_mov_b32 v217, v216\n\t" \
    "s_nop 1\n\t" \
    "v_permlane32_swap_b32 v216, v217\n\t" \
    "s_nop 1\n\t" \
    "v_max_f32 v216, v216, v217\n\t" \
    "v_sub_f32 v176, 0, v216\n\t" \
    "v_sub_f32 v177, 0, v216\n\t" \
    "v_sub_f32 v178, 0, v216\n\t" \
    "v_sub_f32 v179, 0, v216\n\t" \
    "v_sub_f32 v180, 0, v216\n\t" \
    "v_sub_f32 v181, 0, v216\n\t" \
    "v_sub_f32 v182, 0, v216\n\t" \
    "v_sub_f32 v183, 0, v216\n\t" \
    "v_sub_f32 v184, 0, v216\n\t" \
    "v_sub_f32 v185, 0, v216\n\t" \
    "v_sub_f32 v186, 0, v216\n\t" \
    "v_sub_f32 v187, 0, v216\n\t" \
    "v_sub_f32 v188, 0, v216\n\t" \
    "v_sub_f32 v189, 0, v216\n\t" \
    "v_sub_f32 v190, 0, v216\n\t" \
    "v_sub_f32 v191, 0, v216\n\t" \
    "v_exp_f32 v128, v128\n\t" \
    "v_exp_f32 v129, v129\n\t" \
    "s_mov_b32 %[cnt], 0\n\t" \
    "s_mov_b32 %[early], 0\n\t" \
    "1:\n\t" \
    "v_mfma_f32_32x32x16_bf16 %[OY], v[212:215], v[196:199], %[OY]\n\t" \
    "ds_read_b128 v[212:215], %[cur] offset:3072\n\t" \
    "v_exp_f32 v130, v130\n\t" \
    "v_exp_f32 v131, v131\n\t" \
    "v_exp_f32 v132, v132\n\t" \
    "v_mfma_f32_16x16x32_bf16 %[LY], %[ones], v[196:199], %[LY]\n\t" \
    "v_exp_f32 v133, v133\n\t" \
    "v_mfma_f32_32x32x16_bf16 v[144:159], v[200:203], %[qy0], v[176:191]\n\t" \
    "ds_read_b128 v[200:203], %[cur] offset:4096\n\t" \
    "v_exp_f32 v134, v134\n\t" \
    "v_exp_f32 v135, v135\n\t" \
    "v_cvt_pk_bf16_f32 v192, v128, v129\n\t" \
    "v_cvt_pk_bf16_f32 v193, v130, v131\n\t" \
    "v_mfma_f32_32x32x16_bf16 v[144:159], v[204:207], %[qy1], v[144:159]\n\t" \
    "ds_read_b128 v[204:207], %[cur] offset:5120\n\t" \
    "v_cvt_pk_bf16_f32 v194, v132, v133\n\t" \
    "v_cvt_pk_bf16_f32 v195, v134, v135\n\t" \
    "v_exp_f32 v136, v136\n\t" \
    "v_exp_f32 v137, v137\n\t" \
    "s_waitcnt lgkmcnt(3)\n\t" \
    "v_mfma_f32_32x32x16_bf16 %[OX], v[208:211], v[192:195], %[OX]\n\t" \
    "v_exp_f32 v138, v138\n\t" \
    "v_exp_f32 v139, v139\n\t" \
    "v_exp_f32 v140, v140\n\t" \
    "v_mfma_f32_16x16x32_bf16 %[LX], %[ones], v[192:195], %[LX]\n\t" \
    "v_exp_f32 v141, v141\n\t" \
    "v_exp_f32 v142, v142\n\t" \
    "v_exp_f32 v143, v143\n\t" \
    "v_cvt_pk_bf16_f32 v196, v136, v137\n\t" \
    "v_cvt_pk_bf16_f32 v197, v138, v139\n\t" \
    "v_cvt_pk_bf16_f32 v198, v140, v141\n\t" \
    "v_cvt_pk_bf16_f32 v199, v142, v143\n\t" \
    "v_exp_f32 v144, v144\n\t" \
    "v_exp_f32 v145, v145\n\t" \
    "s_waitcnt lgkmcnt(0)\n\t" \
    "v_mfma_f32_32x32x16_bf16 %[OX], v[212:215], v[196:199], %[OX]\n\t" \
    "v_exp_f32 v146, v146\n\t" \
    "v_exp_f32 v147, v147\n\t" \
    "v_exp_f32 v148, v148\n\t" \
    "v_mfma_f32_16x16x32_bf16 %[LX], %[ones], v[196:199], %[LX]\n\t" \
    "v_exp_f32 v149, v149\n\t" \
    "v_mfma_f32_32x32x16_bf16 v[128:143], v[200:203], %[qx0], v[160:175]\n\t" \
    "v_exp_f32 v150, v150\n\t" \
    "v_exp_f32 v151, v151\n\t" \
    "v_cvt_pk_bf16_f32 v192, v144, v145\n\t" \
    "v_cvt_pk_bf16_f32 v193, v146, v147\n\t" \
    "v_mfma_f32_32x32x16_bf16 v[128:143], v[204:207], %[qx1], v[128:143]\n\t" \
    "v_cvt_pk_bf16_f32 v194, v148, v149\n\t" \
    "v_cvt_pk_bf16_f32 v195, v150, v151\n\t" \
    "v_exp_f32 v152, v152\n\t" \
    "v_exp_f32 v153, v153\n\t" \
    "v_mfma_f32_32x32x16_bf16 %[OY], v[208:211], v[192:195], %[OY]\n\t" \
    "ds_read_b128 v[208:211], %[cur] offset:6144\n\t" \
    "v_exp_f32 v154, v154\n\t" \
    "v_exp_f32 v155, v155\n\t" \
    "v_exp_f32 v156, v156\n\t" \
    "v_mfma_f32_16x16x32_bf16 %[LY], %[ones], v[192:195], %[LY]\n\t" \
    "v_exp_f32 v157, v157\n\t" \
    "v_exp_f32 v158, v158\n\t" \
    "v_exp_f32 v159, v159\n\t" \
    "v_cvt_pk_bf16_f32 v196, v152, v153\n\t" \
    "v_cvt_pk_bf16_f32 v197, v154, v155\n\t" \
    "v_cvt_pk_bf16_f32 v198, v156, v157\n\t" \
    "v_cvt_pk_bf16_f32 v199, v158, v159\n\t" \
    "v_exp_f32 v128, v128\n\t" \
    "v_exp_f32 v129, v129\n\t" \
    "v_mfma_f32_32x32x16_bf16 %[OY], v[212:215], v[196:199], %[OY]\n\t" \
    "ds_read_b128 v[212:215], %[cur] offset:7168\n\t" \
    "v_exp_f32 v130, v130\n\t" \
    "v_exp_f32 v131, v131\n\t" \
    "v_exp_f32 v132, v132\n\t" \
    "v_mfma_f32_16x16x32_bf16 %[LY], %[ones], v[196:199], %[LY]\n\t" \
    "v_exp_f32 v133, v133\n\t" \
    "v_mfma_f32_32x32x16_bf16 v[144:159], v[200:203], %[qy0], v[176:191]\n\t" \
    "ds_read_b128 v[200:203], %[cur] offset:8192\n\t" \
    "v_exp_f32 v134, v134\n\t" \
    "v_exp_f32 v135, v135\n\t" \
    "v_cvt_pk_bf16_f32 v192, v128, v129\n\t" \
    "v_cvt_pk_bf16_f32 v193, v130, v131\n\t" \
    "v_mfma_f32_32x32x16_bf16 v[144:159], v[204:207], %[qy1], v[144:159]\n\t" \
    "ds_read_b128 v[204:207], %[cur] offset:9216\n\t" \
    "v_cvt_pk_bf16_f32 v194, v132, v133\n\t" \
    "v_cvt_pk_bf16_f32 v195, v134, v135\n\t" \
    "v_exp_f32 v136, v136\n\t" \
    "v_exp_f32 v137, v137\n\t" \
    "s_waitcnt lgkmcnt(3)\n\t" \
    "v_mfma_f32_32x32x16_bf16 %[OX], v[208:211], v[192:195], %[OX]\n\t" \
    "v_exp_f32 v138, v138\n\t" \
    "v_exp_f32 v139, v139\n\t" \
    "v_exp_f32 v140, v140\n\t" \
    "v_mfma_f32_16x16x32_bf16 %[LX], %[ones], v[192:195], %[LX]\n\t" \
    "v_exp_f32 v141, v141\n\t" \
    "v_exp_f32 v142, v142\n\t" \
    "v_exp_f32 v143, v143\n\t" \
    "v_cvt_pk_bf16_f32 v196, v136, v137\n\t" \
    "v_cvt_pk_bf16_f32 v197, v138, v139\n\t" \
    "v_cvt_pk_bf16_f32 v198, v140, v141\n\t" \
    "v_cvt_pk_bf16_f32 v199, v142, v143\n\t" \
    "v_exp_f32 v144, v144\n\t" \
    "v_exp_f32 v145, v145\n\t" \
    "s_waitcnt lgkmcnt(0)\n\t" \
    "v_mfma_f32_32x32x16_bf16 %[OX], v[212:215], v[196:199], %[OX]\n\t" \
    "v_exp_f32 v146, v146\n\t" \
    "v_exp_f32 v147, v147\n\t" \
    "v_exp_f32 v148, v148\n\t" \
    "v_mfma_f32_16x16x32_bf16 %[LX], %[ones], v[196:199], %[LX]\n\t" \
    "v_exp_f32 v149, v149\n\t" \
    "v_mfma_f32_32x32x16_bf16 v[128:143], v[200:203], %[qx0], v[160:175]\n\t" \
    "v_exp_f32 v150, v150\n\t" \
    "v_exp_f32 v151, v151\n\t" \
    "v_cvt_pk_bf16_f32 v192, v144, v145\n\t" \
    "v_cvt_pk_bf16_f32 v193, v146, v147\n\t" \
    "v_mfma_f32_32x32x16_bf16 v[128:143], v[204:207], %[qx1], v[128:143]\n\t" \
    "v_cvt_pk_bf16_f32 v194, v148, v149\n\t" \
    "v_cvt_pk_bf16_f32 v195, v150, v151\n\t" \
    "v_exp_f32 v152, v152\n\t" \
    "v_exp_f32 v153, v153\n\t" \
    "v_mfma_f32_32x32x16_bf16 %[OY], v[208:211], v[192:195], %[OY]\n\t" \
    "ds_read_b128 v[208:211], %[cur] offset:10240\n\t" \
    "v_exp_f32 v154, v154\n\t" \
    "v_exp_f32 v155, v155\n\t" \
    "v_exp_f32 v156, v156\n\t" \
    "v_mfma_f32_16x16x32_bf16 %[LY], %[ones], v[192:195], %[LY]\n\t" \
    "v_exp_f32 v157, v157\n\t" \
    "v_exp_f32 v158, v158\n\t" \
    "v_exp_f32 v159, v159\n\t" \
    "v_cvt_pk_bf16_f32 v196, v152, v153\n\t" \
    "v_cvt_pk_bf16_f32 v197, v154, v155\n\t" \
    "v_cvt_pk_bf16_f32 v198, v156, v157\n\t" \
    "v_cvt_pk_bf16_f32 v199, v158, v159\n\t" \
    "v_exp_f32 v128, v128\n\t" \
    "v_exp_f32 v129, v129\n\t" \
    "v_mfma_f32_32x32x16_bf16 %[OY], v[212:215], v[196:199], %[OY]\n\t" \
    "ds_read_b128 v[212:215], %[cur] offset:11264\n\t" \
    "v_exp_f32 v130, v130\n\t" \
    "v_exp_f32 v131, v131\n\t" \
    "v_exp_f32 v132, v132\n\t" \
    "v_mfma_f32_16x16x32_bf16 %[LY], %[ones], v[196:199], %[LY]\n\t" \
    "v_exp_f32 v133, v133\n\t" \
    "v_mfma_f32_32x32x16_bf16 v[144:159], v[200:203], %[qy0], v[176:191]\n\t" \
    "ds_read_b128 v[200:203], %[cur] offset:12288\n\t" \
    "v_exp_f32 v134, v134\n\t" \
    "v_exp_f32 v135, v135\n\t" \
    "v_cvt_pk_bf16_f32 v192, v128, v129\n\t" \
    "v_cvt_pk_bf16_f32 v193, v130, v131\n\t" \
    "v_mfma_f32_32x32x16_bf16 v[144:159], v[204:207], %[qy1], v[144:159]\n\t" \
    "ds_read_b128 v[204:207], %[cur] offset:13312\n\t" \
    "v_cvt_pk_bf16_f32 v194, v132, v133\n\t" \
    "v_cvt_pk_bf16_f32 v195, v134, v135\n\t" \
    "v_exp_f32 v136, v136\n\t" \
    "v_exp_f32 v137, v137\n\t" \
    "s_waitcnt lgkmcnt(3)\n\t" \
    "v_mfma_f32_32x32x16_bf16 %[OX], v[208:211], v[192:195], %[OX]\n\t" \
    "v_exp_f32 v138, v138\n\t" \
    "v_exp_f32 v139, v139\n\t" \
    "v_exp_f32 v140, v140\n\t" \
    "v_mfma_f32_16x16x32_bf16 %[LX], %[ones], v[192:195], %[LX]\n\t" \
    "v_exp_f32 v141, v141\n\t" \
    "v_exp_f32 v142, v142\n\t" \
    "v_exp_f32 v143, v143\n\t" \
    "v_cvt_pk_bf16_f32 v196, v136, v137\n\t" \
    "v_cvt_pk_bf16_f32 v197, v138, v139\n\t" \
    "v_cvt_pk_bf16_f32 v198, v140, v141\n\t" \
    "v_cvt_pk_bf16_f32 v199, v142, v143\n\t" \
    "v_exp_f32 v144, v144\n\t" \
    "v_exp_f32 v145, v145\n\t" \
    "s_waitcnt lgkmcnt(0)\n\t" \
    "v_mfma_f32_32x32x16_bf16 %[OX], v[212:215], v[196:199], %[OX]\n\t" \
    "v_exp_f32 v146, v146\n\t" \
    "v_exp_f32 v147, v147\n\t" \
    "v_exp_f32 v148, v148\n\t" \
    "v_mfma_f32_16x16x32_bf16 %[LX], %[ones], v[196:199], %[LX]\n\t" \
    "v_exp_f32 v149, v149\n\t" \
    "v_mfma_f32_32x32x16_bf16 v[128:143], v[200:203], %[qx0], v[160:175]\n\t" \
    "v_exp_f32 v150, v150\n\t" \
    "v_exp_f32 v151, v151\n\t" \
    "v_cvt_pk_bf16_f32 v192, v144, v145\n\t" \
    "v_cvt_pk_bf16_f32 v193, v146, v147\n\t" \
    "v_mfma_f32_32x32x16_bf16 v[128:143], v[204:207], %[qx1], v[128:143]\n\t" \
    "v_cvt_pk_bf16_f32 v194, v148, v149\n\t" \
    "v_cvt_pk_bf16_f32 v195, v150, v151\n\t" \
    "v_exp_f32 v152, v152\n\t" \
    "v_exp_f32 v153, v153\n\t" \
    "v_mfma_f32_32x32x16_bf16 %[OY], v[208:211], v[192:195], %[OY]\n\t" \
    "ds_read_b128 v[208:211], %[cur] offset:14336\n\t" \
    "v_exp_f32 v154, v154\n\t" \
    "v_exp_f32 v155, v155\n\t" \
    "v_exp_f32 v156, v156\n\t" \
    "v_mfma_f32_16x16x32_bf16 %[LY], %[ones], v[192:195], %[LY]\n\t" \
    "v_exp_f32 v157, v157\n\t" \
    "v_exp_f32 v158, v158\n\t" \
    "v_exp_f32 v159, v159\n\t" \
    "v_cvt_pk_bf16_f32 v196, v152, v153\n\t" \
    "v_cvt_pk_bf16_f32 v197, v154, v155\n\t" \
    "v_cvt_pk_bf16_f32 v198, v156, v157\n\t" \
    "v_cvt_pk_bf16_f32 v199, v158, v159\n\t" \
    "v_exp_f32 v128, v128\n\t" \
    "v_exp_f32 v129, v129\n\t" \
    "v_mfma_f32_32x32x16_bf16 %[OY], v[212:215], v[196:199], %[OY]\n\t" \
    "ds_read_b128 v[212:215], %[cur] offset:15360\n\t" \
    "v_exp_f32 v130, v130\n\t" \
    "v_exp_f32 v131, v131\n\t" \
    "v_exp_f32 v132, v132\n\t" \
    "v_mfma_f32_16x16x32_bf16 %[LY], %[ones], v[196:199], %[LY]\n\t" \
    "v_exp_f32 v133, v133\n\t" \
    "s_waitcnt vmcnt(0)\n\t" \
    "s_barrier\n\t" \
    "s_cmp_lg_u32 %[cnt], 5\n\t" \
    "s_cbranch_scc1 6f\n\t" \
    "buffer_load_dwordx4 v[220:223], %[vfb], %[srd], 0 offen sc1\n\t" \
    "6:\n\t" \
    "v_mfma_f32_32x32x16_bf16 v[144:159], v[200:203], %[qy0], v[176:191]\n\t" \
    "ds_read_b128 v[200:203], %[nxt] offset:0\n\t" \
    "v_exp_f32 v134, v134\n\t" \
    "v_exp_f32 v135, v135\n\t" \
    "v_cvt_pk_bf16_f32 v192, v128, v129\n\t" \
    "v_cvt_pk_bf16_f32 v193, v130, v131\n\t" \
    "v_mfma_f32_32x32x16_bf16 v[144:159], v[204:207], %[qy1], v[144:159]\n\t" \
    "ds_read_b128 v[204:207], %[nxt] offset:1024\n\t" \
    "v_cvt_pk_bf16_f32 v194, v132, v133\n\t" \
    "v_cvt_pk_bf16_f32 v195, v134, v135\n\t" \
    "v_exp_f32 v136, v136\n\t" \
    "v_exp_f32 v137, v137\n\t" \
    "s_waitcnt lgkmcnt(3)\n\t" \
    "v_mfma_f32_32x32x16_bf16 %[OX], v[208:211], v[192:195], %[OX]\n\t" \
    "v_exp_f32 v138, v138\n\t" \
    "v_exp_f32 v139, v139\n\t" \
    "v_exp_f32 v140, v140\n\t" \
    "v_mfma_f32_16x16x32_bf16 %[LX], %[ones], v[192:195], %[LX]\n\t" \
    "v_exp_f32 v141, v141\n\t" \
    "v_exp_f32 v142, v142\n\t" \
    "v_exp_f32 v143, v143\n\t" \
    "v_cvt_pk_bf16_f32 v196, v136, v137\n\t" \
    "v_cvt_pk_bf16_f32 v197, v138, v139\n\t" \
    "v_cvt_pk_bf16_f32 v198, v140, v141\n\t" \
    "v_cvt_pk_bf16_f32 v199, v142, v143\n\t" \
    "v_exp_f32 v144, v144\n\t" \
    "v_exp_f32 v145, v145\n\t" \
    "s_waitcnt lgkmcnt(0)\n\t" \
    "v_mfma_f32_32x32x16_bf16 %[OX], v[212:215], v[196:199], %[OX]\n\t" \
    "v_exp_f32 v146, v146\n\t" \
    "v_exp_f32 v147, v147\n\t" \
    "v_exp_f32 v148, v148\n\t" \
    "v_mfma_f32_16x16x32_bf16 %[LX], %[ones], v[196:199], %[LX]\n\t" \
    "v_exp_f32 v149, v149\n\t" \
    "v_mfma_f32_32x32x16_bf16 v[128:143], v[200:203], %[qx0], v[160:175]\n\t" \
    "v_exp_f32 v150, v150\n\t" \
    "v_exp_f32 v151, v151\n\t" \
    "v_cvt_pk_bf16_f32 v192, v144, v145\n\t" \
    "v_cvt_pk_bf16_f32 v193, v146, v147\n\t" \
    "v_mfma_f32_32x32x16_bf16 v[128:143], v[204:207], %[qx1], v[128:143]\n\t" \
    "v_cvt_pk_bf16_f32 v194, v148, v149\n\t" \
    "v_cvt_pk_bf16_f32 v195, v150, v151\n\t" \
    "v_exp_f32 v152, v152\n\t" \
    "v_exp_f32 v153, v153\n\t" \
    "v_mfma_f32_32x32x16_bf16 %[OY], v[208:211], v[192:195], %[OY]\n\t" \
    "ds_read_b128 v[208:211], %[nxt] offset:2048\n\t" \
    "v_exp_f32 v154, v154\n\t" \
    "v_exp_f32 v155, v155\n\t" \
    "v_exp_f32 v156, v156\n\t" \
    "v_mfma_f32_16x16x32_bf16 %[LY], %[ones], v[192:195], %[LY]\n\t" \
    "v_exp_f32 v157, v157\n\t" \
    "v_exp_f32 v158, v158\n\t" \
    "v_exp_f32 v159, v159\n\t" \
    "v_cvt_pk_bf16_f32 v196, v152, v153\n\t" \
    "v_cvt_pk_bf16_f32 v197, v154, v155\n\t" \
    "v_cvt_pk_bf16_f32 v198, v156, v157\n\t" \
    "v_cvt_pk_bf16_f32 v199, v158, v159\n\t" \
    "v_exp_f32 v128, v128\n\t" \
    "v_exp_f32 v129, v129\n\t" \
    "v_mov_b32 %[cur], %[nxt]\n\t" \
    "v_add_u32 %[nxt], %[sfar], %[vgrp]\n\t" \
    "s_add_u32 %[tmp], %[sfar], 0x8000\n\t" \
    "s_cmp_eq_u32 %[sfar], 0x20000\n\t" \
    "s_cselect_b32 %[sfar], 0x10000, %[tmp]\n\t" \
    "s_add_u32 %[cnt], %[cnt], 1\n\t" \
    "s_cmp_lt_u32 %[cnt], 6\n\t" \
    "s_cbranch_scc1 1b\n\t" \
    "v_mfma_f32_32x32x16_bf16 %[OY], v[212:215], v[196:199], %[OY]\n\t" \
    "ds_read_b128 v[212:215], %[cur] offset:3072\n\t" \
    "v_exp_f32 v130, v130\n\t" \
    "v_exp_f32 v131, v131\n\t" \
    "v_exp_f32 v132, v132\n\t" \
    "v_mfma_f32_16x16x32_bf16 %[LY], %[ones], v[196:199], %[LY]\n\t" \
    "v_exp_f32 v133, v133\n\t" \
    "v_mfma_f32_32x32x16_bf16 v[144:159], v[200:203], %[qy0], v[176:191]\n\t" \
    "ds_read_b128 v[200:203], %[cur] offset:4096\n\t" \
    "v_exp_f32 v134, v134\n\t" \
    "v_exp_f32 v135, v135\n\t" \
    "v_cvt_pk_bf16_f32 v192, v128, v129\n\t" \
    "v_cvt_pk_bf16_f32 v193, v130, v131\n\t" \
    "v_mfma_f32_32x32x16_bf16 v[144:159], v[204:207], %[qy1], v[144:159]\n\t" \
    "ds_read_b128 v[204:207], %[cur] offset:5120\n\t" \
    "v_cvt_pk_bf16_f32 v194, v132, v133\n\t" \
    "v_cvt_pk_bf16_f32 v195, v134, v135\n\t" \
    "v_exp_f32 v136, v136\n\t" \
    "v_exp_f32 v137, v137\n\t" \
    "s_waitcnt lgkmcnt(3)\n\t" \
    "v_mfma_f32_32x32x16_bf16 %[OX], v[208:211], v[192:195], %[OX]\n\t" \
    "v_exp_f32 v138, v138\n\t" \
    "v_exp_f32 v139, v139\n\t" \
    "v_exp_f32 v140, v140\n\t" \
    "v_mfma_f32_16x16x32_bf16 %[LX], %[ones], v[192:195], %[LX]\n\t" \
    "v_exp_f32 v141, v141\n\t" \
    "v_exp_f32 v142, v142\n\t" \
    "v_exp_f32 v143, v143\n\t" \
    "v_cvt_pk_bf16_f32 v196, v136, v137\n\t" \
    "v_cvt_pk_bf16_f32 v197, v138, v139\n\t" \
    "v_cvt_pk_bf16_f32 v198, v140, v141\n\t" \
    "v_cvt_pk_bf16_f32 v199, v142, v143\n\t" \
    "v_exp_f32 v144, v144\n\t" \
    "v_exp_f32 v145, v145\n\t" \
    "s_waitcnt lgkmcnt(0)\n\t" \
    "v_mfma_f32_32x32x16_bf16 %[OX], v[212:215], v[196:199], %[OX]\n\t" \
    "v_exp_f32 v146, v146\n\t" \
    "v_exp_f32 v147, v147\n\t" \
    "v_exp_f32 v148, v148\n\t" \
    "v_mfma_f32_16x16x32_bf16 %[LX], %[ones], v[196:199], %[LX]\n\t" \
    "v_exp_f32 v149, v149\n\t" \
    "v_mfma_f32_32x32x16_bf16 v[128:143], v[200:203], %[qx0], v[160:175]\n\t" \
    "v_exp_f32 v150, v150\n\t" \
    "v_exp_f32 v151, v151\n\t" \
    "v_cvt_pk_bf16_f32 v192, v144, v145\n\t" \
    "v_cvt_pk_bf16_f32 v193, v146, v147\n\t" \
    "v_mfma_f32_32x32x16_bf16 v[128:143], v[204:207], %[qx1], v[128:143]\n\t" \
    "v_cvt_pk_bf16_f32 v194, v148, v149\n\t" \
    "v_cvt_pk_bf16_f32 v195, v150, v151\n\t" \
    "v_exp_f32 v152, v152\n\t" \
    "v_exp_f32 v153, v153\n\t" \
    "v_mfma_f32_32x32x16_bf16 %[OY], v[208:211], v[192:195], %[OY]\n\t" \
    "ds_read_b128 v[208:211], %[cur] offset:6144\n\t" \
    "v_exp_f32 v154, v154\n\t" \
    "v_exp_f32 v155, v155\n\t" \
    "v_exp_f32 v156, v156\n\t" \
    "v_mfma_f32_16x16x32_bf16 %[LY], %[ones], v[192:195], %[LY]\n\t" \
    "v_exp_f32 v157, v157\n\t" \
    "v_exp_f32 v158, v158\n\t" \
    "v_exp_f32 v159, v159\n\t" \
    "v_cvt_pk_bf16_f32 v196, v152, v153\n\t" \
    "v_cvt_pk_bf16_f32 v197, v154, v155\n\t" \
    "v_cvt_pk_bf16_f32 v198, v156, v157\n\t" \
    "v_cvt_pk_bf16_f32 v199, v158, v159\n\t" \
    "v_exp_f32 v128, v128\n\t" \
    "v_exp_f32 v129, v129\n\t" \
    "v_mfma_f32_32x32x16_bf16 %[OY], v[212:215], v[196:199], %[OY]\n\t" \
    "ds_read_b128 v[212:215], %[cur] offset:7168\n\t" \
    "v_exp_f32 v130, v130\n\t" \
    "v_exp_f32 v131, v131\n\t" \
    "v_exp_f32 v132, v132\n\t" \
    "v_mfma_f32_16x16x32_bf16 %[LY], %[ones], v[196:199], %[LY]\n\t" \
    "v_exp_f32 v133, v133\n\t" \
    "v_mfma_f32_32x32x16_bf16 v[144:159], v[200:203], %[qy0], v[176:191]\n\t" \
    "ds_read_b128 v[200:203], %[cur] offset:8192\n\t" \
    "v_exp_f32 v134, v134\n\t" \
    "v_exp_f32 v135, v135\n\t" \
    "v_cvt_pk_bf16_f32 v192, v128, v129\n\t" \
    "v_cvt_pk_bf16_f32 v193, v130, v131\n\t" \
    "v_mfma_f32_32x32x16_bf16 v[144:159], v[204:207], %[qy1], v[144:159]\n\t" \
    "ds_read_b128 v[204:207], %[cur] offset:9216\n\t" \
    "v_cvt_pk_bf16_f32 v194, v132, v133\n\t" \
    "v_cvt_pk_bf16_f32 v195, v134, v135\n\t" \
    "v_exp_f32 v136, v136\n\t" \
    "v_exp_f32 v137, v137\n\t" \
    "s_waitcnt lgkmcnt(3)\n\t" \
    "v_mfma_f32_32x32x16_bf16 %[OX], v[208:211], v[192:195], %[OX]\n\t" \
    "v_exp_f32 v138, v138\n\t" \
    "v_exp_f32 v139, v139\n\t" \
    "v_exp_f32 v140, v140\n\t" \
    "v_mfma_f32_16x16x32_bf16 %[LX], %[ones], v[192:195], %[LX]\n\t" \
    "v_exp_f32 v141, v141\n\t" \
    "v_exp_f32 v142, v142\n\t" \
    "v_exp_f32 v143, v143\n\t" \
    "v_cvt_pk_bf16_f32 v196, v136, v137\n\t" \
    "v_cvt_pk_bf16_f32 v197, v138, v139\n\t" \
    "v_cvt_pk_bf16_f32 v198, v140, v141\n\t" \
    "v_cvt_pk_bf16_f32 v199, v142, v143\n\t" \
    "v_exp_f32 v144, v144\n\t" \
    "v_exp_f32 v145, v145\n\t" \
    "s_waitcnt lgkmcnt(0)\n\t" \
    "v_mfma_f32_32x32x16_bf16 %[OX], v[212:215], v[196:199], %[OX]\n\t" \
    "v_exp_f32 v146, v146\n\t" \
    "v_exp_f32 v147, v147\n\t" \
    "v_exp_f32 v148, v148\n\t" \
    "v_mfma_f32_16x16x32_bf16 %[LX], %[ones], v[196:199], %[LX]\n\t" \
    "v_exp_f32 v149, v149\n\t" \
    "v_mfma_f32_32x32x16_bf16 v[128:143], v[200:203], %[qx0], v[160:175]\n\t" \
    "v_exp_f32 v150, v150\n\t" \
    "v_exp_f32 v151, v151\n\t" \
    "v_cvt_pk_bf16_f32 v192, v144, v145\n\t" \
    "v_cvt_pk_bf16_f32 v193, v146, v147\n\t" \
    "v_mfma_f32_32x32x16_bf16 v[128:143], v[204:207], %[qx1], v[128:143]\n\t" \
    "v_cvt_pk_bf16_f32 v194, v148, v149\n\t" \
    "v_cvt_pk_bf16_f32 v195, v150, v151\n\t" \
    "v_exp_f32 v152, v152\n\t" \
    "v_exp_f32 v153, v153\n\t" \
    "v_mfma_f32_32x32x16_bf16 %[OY], v[208:211], v[192:195], %[OY]\n\t" \
    "ds_read_b128 v[208:211], %[cur] offset:10240\n\t" \
    "v_exp_f32 v154, v154\n\t" \
    "v_exp_f32 v155, v155\n\t" \
    "v_exp_f32 v156, v156\n\t" \
    "v_mfma_f32_16x16x32_bf16 %[LY], %[ones], v[192:195], %[LY]\n\t" \
    "v_exp_f32 v157, v157\n\t" \
    "v_exp_f32 v158, v158\n\t" \
    "v_exp_f32 v159, v159\n\t" \
    "v_cvt_pk_bf16_f32 v196, v152, v153\n\t" \
    "v_cvt_pk_bf16_f32 v197, v154, v155\n\t" \
    "v_cvt_pk_bf16_f32 v198, v156, v157\n\t" \
    "v_cvt_pk_bf16_f32 v199, v158, v159\n\t" \
    "v_exp_f32 v128, v128\n\t" \
    "v_exp_f32 v129, v129\n\t" \
    "v_mfma_f32_32x32x16_bf16 %[OY], v[212:215], v[196:199], %[OY]\n\t" \
    "ds_read_b128 v[212:215], %[cur] offset:11264\n\t" \
    "v_exp_f32 v130, v130\n\t" \
    "v_exp_f32 v131, v131\n\t" \
    "v_exp_f32 v132, v132\n\t" \
    "v_mfma_f32_16x16x32_bf16 %[LY], %[ones], v[196:199], %[LY]\n\t" \
    "v_exp_f32 v133, v133\n\t" \
    "v_mfma_f32_32x32x16_bf16 v[144:159], v[200:203], %[qy0], v[176:191]\n\t" \
    "ds_read_b128 v[200:203], %[cur] offset:12288\n\t" \
    "v_exp_f32 v134, v134\n\t" \
    "v_exp_f32 v135, v135\n\t" \
    "v_cvt_pk_bf16_f32 v192, v128, v129\n\t" \
    "v_cvt_pk_bf16_f32 v193, v130, v131\n\t" \
    "v_mfma_f32_32x32x16_bf16 v[144:159], v[204:207], %[qy1], v[144:159]\n\t" \
    "ds_read_b128 v[204:207], %[cur] offset:13312\n\t" \
    "v_cvt_pk_bf16_f32 v194, v132, v133\n\t" \
    "v_cvt_pk_bf16_f32 v195, v134, v135\n\t" \
    "v_exp_f32 v136, v136\n\t" \
    "v_exp_f32 v137, v137\n\t" \
    "s_waitcnt lgkmcnt(3)\n\t" \
    "v_mfma_f32_32x32x16_bf16 %[OX], v[208:211], v[192:195], %[OX]\n\t" \
    "v_exp_f32 v138, v138\n\t" \
    "v_exp_f32 v139, v139\n\t" \
    "v_exp_f32 v140, v140\n\t" \
    "v_mfma_f32_16x16x32_bf16 %[LX], %[ones], v[192:195], %[LX]\n\t" \
    "v_exp_f32 v141, v141\n\t" \
    "v_exp_f32 v142, v142\n\t" \
    "v_exp_f32 v143, v143\n\t" \
    "v_cvt_pk_bf16_f32 v196, v136, v137\n\t" \
    "v_cvt_pk_bf16_f32 v197, v138, v139\n\t" \
    "v_cvt_pk_bf16_f32 v198, v140, v141\n\t" \
    "v_cvt_pk_bf16_f32 v199, v142, v143\n\t" \
    "v_exp_f32 v144, v144\n\t" \
    "v_exp_f32 v145, v145\n\t" \
    "s_waitcnt lgkmcnt(0)\n\t" \
    "v_mfma_f32_32x32x16_bf16 %[OX], v[212:215], v[196:199], %[OX]\n\t" \
    "v_exp_f32 v146, v146\n\t" \
    "v_exp_f32 v147, v147\n\t" \
    "v_exp_f32 v148, v148\n\t" \
    "v_mfma_f32_16x16x32_bf16 %[LX], %[ones], v[196:199], %[LX]\n\t" \
    "v_exp_f32 v149, v149\n\t" \
    "v_mfma_f32_32x32x16_bf16 v[128:143], v[200:203], %[qx0], v[160:175]\n\t" \
    "v_exp_f32 v150, v150\n\t" \
    "v_exp_f32 v151, v151\n\t" \
    "v_cvt_pk_bf16_f32 v192, v144, v145\n\t" \
    "v_cvt_pk_bf16_f32 v193, v146, v147\n\t" \
    "v_mfma_f32_32x32x16_bf16 v[128:143], v[204:207], %[qx1], v[128:143]\n\t" \
    "v_cvt_pk_bf16_f32 v194, v148, v149\n\t" \
    "v_cvt_pk_bf16_f32 v195, v150, v151\n\t" \
    "v_exp_f32 v152, v152\n\t" \
    "v_exp_f32 v153, v153\n\t" \
    "v_mfma_f32_32x32x16_bf16 %[OY], v[208:211], v[192:195], %[OY]\n\t" \
    "ds_read_b128 v[208:211], %[cur] offset:14336\n\t" \
    "v_exp_f32 v154, v154\n\t" \
    "v_exp_f32 v155, v155\n\t" \
    "v_exp_f32 v156, v156\n\t" \
    "v_mfma_f32_16x16x32_bf16 %[LY], %[ones], v[192:195], %[LY]\n\t" \
    "v_exp_f32 v157, v157\n\t" \
    "v_exp_f32 v158, v158\n\t" \
    "v_exp_f32 v159, v159\n\t" \
    "v_cvt_pk_bf16_f32 v196, v152, v153\n\t" \
    "v_cvt_pk_bf16_f32 v197, v154, v155\n\t" \
    "v_cvt_pk_bf16_f32 v198, v156, v157\n\t" \
    "v_cvt_pk_bf16_f32 v199, v158, v159\n\t" \
    "v_exp_f32 v128, v128\n\t" \
    "v_exp_f32 v129, v129\n\t" \
    "v_mfma_f32_32x32x16_bf16 %[OY], v[212:215], v[196:199], %[OY]\n\t" \
    "ds_read_b128 v[212:215], %[cur] offset:15360\n\t" \
    "v_exp_f32 v130, v130\n\t" \
    "v_exp_f32 v131, v131\n\t" \
    "v_exp_f32 v132, v132\n\t" \
    "v_mfma_f32_16x16x32_bf16 %[LY], %[ones], v[196:199], %[LY]\n\t" \
    "v_exp_f32 v133, v133\n\t" \
    "s_waitcnt vmcnt(0)\n\t" \
    "s_barrier\n\t" \
    "v_cmp_eq_u32 vcc, %[vtag], v220\n\t" \
    "v_cmp_eq_u32 s[40:41], %[vtag], v221\n\t" \
    "s_and_b64 s[40:41], s[40:41], vcc\n\t" \
    "v_cmp_eq_u32 vcc, %[vtag], v222\n\t" \
    "s_and_b64 s[40:41], s[40:41], vcc\n\t" \
    "v_cmp_eq_u32 vcc, %[vtag], v223\n\t" \
    "s_and_b64 s[40:41], s[40:41], vcc\n\t" \
    "s_cmp_eq_u64 s[40:41], exec\n\t" \
    "s_cselect_b32 %[early], 1, 0\n\t" \
    "v_mfma_f32_32x32x16_bf16 v[144:159], v[200:203], %[qy0], v[176:191]\n\t" \
    "ds_read_b128 v[200:203], %[nxt] offset:0\n\t" \
    "s_cmp_lg_u32 %[early], 0\n\t" \
    "s_cbranch_scc0 91f\n\t" \
    "s_mov_b32 m0, 0xe000\n\t" \
    "s_nop 0\n\t" \
    "buffer_load_dword %[vopf], %[srd], 0 offen lds\n\t" \
    "s_mov_b32 m0, %[emo]\n\t" \
    "s_nop 0\n\t" \
    "buffer_load_dwordx4 %[vlane], %[srd], %[eos] offen offset:0 sc1 lds\n\t" \
    "buffer_load_dwordx4 %[vlane], %[srd], %[eos] offen offset:1024 sc1 lds\n\t" \
    "91:\n\t" \
    "v_exp_f32 v134, v134\n\t" \
    "v_exp_f32 v135, v135\n\t" \
    "v_cvt_pk_bf16_f32 v192, v128, v129\n\t" \
    "v_cvt_pk_bf16_f32 v193, v130, v131\n\t" \
    "v_mfma_f32_32x32x16_bf16 v[144:159], v[204:207], %[qy1], v[144:159]\n\t" \
    "ds_read_b128 v[204:207], %[nxt] offset:1024\n\t" \
    "v_cvt_pk_bf16_f32 v194, v132, v133\n\t" \
    "v_cvt_pk_bf16_f32 v195, v134, v135\n\t" \
    "v_exp_f32 v136, v136\n\t" \
    "v_exp_f32 v137, v137\n\t" \
    "s_waitcnt lgkmcnt(3)\n\t" \
    "v_mfma_f32_32x32x16_bf16 %[OX], v[208:211], v[192:195], %[OX]\n\t" \
    "v_exp_f32 v138, v138\n\t" \
    "v_exp_f32 v139, v139\n\t" \
    "v_exp_f32 v140, v140\n\t" \
    "v_mfma_f32_16x16x32_bf16 %[LX], %[ones], v[192:195], %[LX]\n\t" \
    "v_exp_f32 v141, v141\n\t" \
    "v_exp_f32 v142, v142\n\t" \
    "v_exp_f32 v143, v143\n\t" \
    "v_cvt_pk_bf16_f32 v196, v136, v137\n\t" \
    "v_cvt_pk_bf16_f32 v197, v138, v139\n\t" \
    "v_cvt_pk_bf16_f32 v198, v140, v141\n\t" \
    "v_cvt_pk_bf16_f32 v199, v142, v143\n\t" \
    "v_exp_f32 v144, v144\n\t" \
    "v_exp_f32 v145, v145\n\t" \
    "s_waitcnt lgkmcnt(0)\n\t" \
    "v_mfma_f32_32x32x16_bf16 %[OX], v[212:215], v[196:199], %[OX]\n\t" \
    "v_exp_f32 v146, v146\n\t" \
    "v_exp_f32 v147, v147\n\t" \
    "v_exp_f32 v148, v148\n\t" \
    "v_mfma_f32_16x16x32_bf16 %[LX], %[ones], v[196:199], %[LX]\n\t" \
    "v_exp_f32 v149, v149\n\t" \
    "v_mfma_f32_32x32x16_bf16 v[128:143], v[200:203], %[qx0], v[160:175]\n\t" \
    "s_cmp_lg_u32 %[early], 0\n\t" \
    "s_cbranch_scc0 92f\n\t" \
    "buffer_load_dwordx4 %[vlane], %[srd], %[eos] offen offset:2048 sc1 lds\n\t" \
    "buffer_load_dwordx4 %[vlane], %[srd], %[eos] offen offset:3072 sc1 lds\n\t" \
    "92:\n\t" \
    "v_exp_f32 v150, v150\n\t" \
    "v_exp_f32 v151, v151\n\t" \
    "v_cvt_pk_bf16_f32 v192, v144, v145\n\t" \
    "v_cvt_pk_bf16_f32 v193, v146, v147\n\t" \
    "v_mfma_f32_32x32x16_bf16 v[128:143], v[204:207], %[qx1], v[128:143]\n\t" \
    "v_cvt_pk_bf16_f32 v194, v148, v149\n\t" \
    "v_cvt_pk_bf16_f32 v195, v150, v151\n\t" \
    "v_exp_f32 v152, v152\n\t" \
    "v_exp_f32 v153, v153\n\t" \
    "v_mfma_f32_32x32x16_bf16 %[OY], v[208:211], v[192:195], %[OY]\n\t" \
    "ds_read_b128 v[208:211], %[nxt] offset:2048\n\t" \
    "v_exp_f32 v154, v154\n\t" \
    "v_exp_f32 v155, v155\n\t" \
    "v_exp_f32 v156, v156\n\t" \
    "v_mfma_f32_16x16x32_bf16 %[LY], %[ones], v[192:195], %[LY]\n\t" \
    "v_exp_f32 v157, v157\n\t" \
    "v_exp_f32 v158, v158\n\t" \
    "v_exp_f32 v159, v159\n\t" \
    "v_cvt_pk_bf16_f32 v196, v152, v153\n\t" \
    "v_cvt_pk_bf16_f32 v197, v154, v155\n\t" \
    "v_cvt_pk_bf16_f32 v198, v156, v157\n\t" \
    "v_cvt_pk_bf16_f32 v199, v158, v159\n\t" \
    "v_exp_f32 v128, v128\n\t" \
    "v_exp_f32 v129, v129\n\t" \
    "v_mov_b32 %[cur], %[nxt]\n\t" \
    "v_add_u32 %[nxt], %[sfar], %[vgrp]\n\t" \
    "v_mfma_f32_32x32x16_bf16 %[OY], v[212:215], v[196:199], %[OY]\n\t" \
    "ds_read_b128 v[212:215], %[cur] offset:3072\n\t" \
    "v_exp_f32 v130, v130\n\t" \
    "v_exp_f32 v131, v131\n\t" \
    "v_exp_f32 v132, v132\n\t" \
    "v_mfma_f32_16x16x32_bf16 %[LY], %[ones], v[196:199], %[LY]\n\t" \
    "v_exp_f32 v133, v133\n\t" \
    "v_mfma_f32_32x32x16_bf16 v[144:159], v[200:203], %[qy0], v[176:191]\n\t" \
    "ds_read_b128 v[200:203], %[cur] offset:4096\n\t" \
    "s_cmp_lg_u32 %[early], 0\n\t" \
    "s_cbranch_scc0 93f\n\t" \
    "s_lshr_b32 %[tmp], %[emo], 1\n\t" \
    "s_add_u32 m0, %[tmp], 0x4000\n\t" \
    "s_nop 0\n\t" \
    "buffer_load_dwordx4 %[wv0], %[wob], 0 offen lds\n\t" \
    "s_add_u32 m0, %[tmp], 0x4400\n\t" \
    "s_nop 0\n\t" \
    "buffer_load_dwordx4 %[wv1], %[wob], 0 offen lds\n\t" \
    "93:\n\t" \
    "v_exp_f32 v134, v134\n\t" \
    "v_exp_f32 v135, v135\n\t" \
    "v_cvt_pk_bf16_f32 v192, v128, v129\n\t" \
    "v_cvt_pk_bf16_f32 v193, v130, v131\n\t" \
    "v_mfma_f32_32x32x16_bf16 v[144:159], v[204:207], %[qy1], v[144:159]\n\t" \
    "ds_read_b128 v[204:207], %[cur] offset:5120\n\t" \
    "v_cvt_pk_bf16_f32 v194, v132, v133\n\t" \
    "v_cvt_pk_bf16_f32 v195, v134, v135\n\t" \
    "v_exp_f32 v136, v136\n\t" \
    "v_exp_f32 v137, v137\n\t" \
    "s_waitcnt lgkmcnt(3)\n\t" \
    "v_mfma_f32_32x32x16_bf16 %[OX], v[208:211], v[192:195], %[OX]\n\t" \
    "v_exp_f32 v138, v138\n\t" \
    "v_exp_f32 v139, v139\n\t" \
    "v_exp_f32 v140, v140\n\t" \
    "v_mfma_f32_16x16x32_bf16 %[LX], %[ones], v[192:195], %[LX]\n\t" \
    "v_exp_f32 v141, v141\n\t" \
    "v_exp_f32 v142, v142\n\t" \
    "v_exp_f32 v143, v143\n\t" \
    "v_cvt_pk_bf16_f32 v196, v136, v137\n\t" \
    "v_cvt_pk_bf16_f32 v197, v138, v139\n\t" \
    "v_cvt_pk_bf16_f32 v198, v140, v141\n\t" \
    "v_cvt_pk_bf16_f32 v199, v142, v143\n\t" \
    "v_exp_f32 v144, v144\n\t" \
    "v_exp_f32 v145, v145\n\t" \
    "s_waitcnt lgkmcnt(0)\n\t" \
    "v_mfma_f32_32x32x16_bf16 %[OX], v[212:215], v[196:199], %[OX]\n\t" \
    "v_exp_f32 v146, v146\n\t" \
    "v_exp_f32 v147, v147\n\t" \
    "v_exp_f32 v148, v148\n\t" \
    "v_mfma_f32_16x16x32_bf16 %[LX], %[ones], v[196:199], %[LX]\n\t" \
    "v_exp_f32 v149, v149\n\t" \
    "v_mfma_f32_32x32x16_bf16 v[128:143], v[200:203], %[qx0], v[160:175]\n\t" \
    "s_cmp_lg_u32 %[early], 0\n\t" \
    "s_cbranch_scc0 94f\n\t" \
    "s_add_u32 m0, %[emo], 0x6000\n\t" \
    "s_add_u32 %[tmp], %[eos], 0x1000\n\t" \
    "buffer_load_dwordx4 %[vlane], %[srd], %[tmp] offen offset:0 sc1 lds\n\t" \
    "buffer_load_dwordx4 %[vlane], %[srd], %[tmp] offen offset:1024 sc1 lds\n\t" \
    "94:\n\t" \
    "v_exp_f32 v150, v150\n\t" \
    "v_exp_f32 v151, v151\n\t" \
    "v_cvt_pk_bf16_f32 v192, v144, v145\n\t" \
    "v_cvt_pk_bf16_f32 v193, v146, v147\n\t" \
    "v_mfma_f32_32x32x16_bf16 v[128:143], v[204:207], %[qx1], v[128:143]\n\t" \
    "v_cvt_pk_bf16_f32 v194, v148, v149\n\t" \
    "v_cvt_pk_bf16_f32 v195, v150, v151\n\t" \
    "v_exp_f32 v152, v152\n\t" \
    "v_exp_f32 v153, v153\n\t" \
    "v_mfma_f32_32x32x16_bf16 %[OY], v[208:211], v[192:195], %[OY]\n\t" \
    "ds_read_b128 v[208:211], %[cur] offset:6144\n\t" \
    "v_exp_f32 v154, v154\n\t" \
    "v_exp_f32 v155, v155\n\t" \
    "v_exp_f32 v156, v156\n\t" \
    "v_mfma_f32_16x16x32_bf16 %[LY], %[ones], v[192:195], %[LY]\n\t" \
    "v_exp_f32 v157, v157\n\t" \
    "v_exp_f32 v158, v158\n\t" \
    "v_exp_f32 v159, v159\n\t" \
    "v_cvt_pk_bf16_f32 v196, v152, v153\n\t" \
    "v_cvt_pk_bf16_f32 v197, v154, v155\n\t" \
    "v_cvt_pk_bf16_f32 v198, v156, v157\n\t" \
    "v_cvt_pk_bf16_f32 v199, v158, v159\n\t" \
    "v_exp_f32 v128, v128\n\t" \
    "v_exp_f32 v129, v129\n\t" \
    "v_mfma_f32_32x32x16_bf16 %[OY], v[212:215], v[196:199], %[OY]\n\t" \
    "ds_read_b128 v[212:215], %[cur] offset:7168\n\t" \
    "v_exp_f32 v130, v130\n\t" \
    "v_exp_f32 v131, v131\n\t" \
    "v_exp_f32 v132, v132\n\t" \
    "v_mfma_f32_16x16x32_bf16 %[LY], %[ones], v[196:199], %[LY]\n\t" \
    "v_exp_f32 v133, v133\n\t" \
    "v_mfma_f32_32x32x16_bf16 v[144:159], v[200:203], %[qy0], v[176:191]\n\t" \
    "ds_read_b128 v[200:203], %[cur] offset:8192\n\t" \
    "s_cmp_lg_u32 %[early], 0\n\t" \
    "s_cbranch_scc0 95f\n\t" \
    "s_add_u32 %[tmp], %[eos], 0x1000\n\t" \
    "buffer_load_dwordx4 %[vlane], %[srd], %[tmp] offen offset:2048 sc1 lds\n\t" \
    "buffer_load_dwordx4 %[vlane], %[srd], %[tmp] offen offset:3072 sc1 lds\n\t" \
    "95:\n\t" \
    "v_exp_f32 v134, v134\n\t" \
    "v_exp_f32 v135, v135\n\t" \
    "v_cvt_pk_bf16_f32 v192, v128, v129\n\t" \
    "v_cvt_pk_bf16_f32 v193, v130, v131\n\t" \
    "v_mfma_f32_32x32x16_bf16 v[144:159], v[204:207], %[qy1], v[144:159]\n\t" \
    "ds_read_b128 v[204:207], %[cur] offset:9216\n\t" \
    "v_cvt_pk_bf16_f32 v194, v132, v133\n\t" \
    "v_cvt_pk_bf16_f32 v195, v134, v135\n\t" \
    "v_exp_f32 v136, v136\n\t" \
    "v_exp_f32 v137, v137\n\t" \
    "s_waitcnt lgkmcnt(3)\n\t" \
    "v_mfma_f32_32x32x16_bf16 %[OX], v[208:211], v[192:195], %[OX]\n\t" \
    "v_exp_f32 v138, v138\n\t" \
    "v_exp_f32 v139, v139\n\t" \
    "v_exp_f32 v140, v140\n\t" \
    "v_mfma_f32_16x16x32_bf16 %[LX], %[ones], v[192:195], %[LX]\n\t" \
    "v_exp_f32 v141, v141\n\t" \
    "v_exp_f32 v142, v142\n\t" \
    "v_exp_f32 v143, v143\n\t" \
    "v_cvt_pk_bf16_f32 v196, v136, v137\n\t" \
    "v_cvt_pk_bf16_f32 v197, v138, v139\n\t" \
    "v_cvt_pk_bf16_f32 v198, v140, v141\n\t" \
    "v_cvt_pk_bf16_f32 v199, v142, v143\n\t" \
    "v_exp_f32 v144, v144\n\t" \
    "v_exp_f32 v145, v145\n\t" \
    "s_waitcnt lgkmcnt(0)\n\t" \
    "v_mfma_f32_32x32x16_bf16 %[OX], v[212:215], v[196:199], %[OX]\n\t" \
    "v_exp_f32 v146, v146\n\t" \
    "v_exp_f32 v147, v147\n\t" \
    "v_exp_f32 v148, v148\n\t" \
    "v_mfma_f32_16x16x32_bf16 %[LX], %[ones], v[196:199], %[LX]\n\t" \
    "v_exp_f32 v149, v149\n\t" \
    "v_mfma_f32_32x32x16_bf16 v[128:143], v[200:203], %[qx0], v[160:175]\n\t" \
    "s_cmp_lg_u32 %[early], 0\n\t" \
    "s_cbranch_scc0 96f\n\t" \
    "s_lshr_b32 %[tmp], %[emo], 1\n\t" \
    "s_add_u32 m0, %[tmp], 0xa000\n\t" \
    "s_mov_b32 s40, 0x20000\n\t" \
    "buffer_load_dwordx4 %[wv0], %[wob], s40 offen lds\n\t" \
    "s_add_u32 m0, %[tmp], 0xa400\n\t" \
    "s_nop 0\n\t" \
    "buffer_load_dwordx4 %[wv1], %[wob], s40 offen lds\n\t" \
    "96:\n\t" \
    "v_exp_f32 v150, v150\n\t" \
    "v_exp_f32 v151, v151\n\t" \
    "v_cvt_pk_bf16_f32 v192, v144, v145\n\t" \
    "v_cvt_pk_bf16_f32 v193, v146, v147\n\t" \
    "v_mfma_f32_32x32x16_bf16 v[128:143], v[204:207], %[qx1], v[128:143]\n\t" \
    "v_cvt_pk_bf16_f32 v194, v148, v149\n\t" \
    "v_cvt_pk_bf16_f32 v195, v150, v151\n\t" \
    "v_exp_f32 v152, v152\n\t" \
    "v_exp_f32 v153, v153\n\t" \
    "v_mfma_f32_32x32x16_bf16 %[OY], v[208:211], v[192:195], %[OY]\n\t" \
    "ds_read_b128 v[208:211], %[cur] offset:10240\n\t" \
    "v_exp_f32 v154, v154\n\t" \
    "v_exp_f32 v155, v155\n\t" \
    "v_exp_f32 v156, v156\n\t" \
    "v_mfma_f32_16x16x32_bf16 %[LY], %[ones], v[192:195], %[LY]\n\t" \
    "v_exp_f32 v157, v157\n\t" \
    "v_exp_f32 v158, v158\n\t" \
    "v_exp_f32 v159, v159\n\t" \
    "v_cvt_pk_bf16_f32 v196, v152, v153\n\t" \
    "v_cvt_pk_bf16_f32 v197, v154, v155\n\t" \
    "v_cvt_pk_bf16_f32 v198, v156, v157\n\t" \
    "v_cvt_pk_bf16_f32 v199, v158, v159\n\t" \
    "v_exp_f32 v128, v128\n\t" \
    "v_exp_f32 v129, v129\n\t" \
    "v_mfma_f32_32x32x16_bf16 %[OY], v[212:215], v[196:199], %[OY]\n\t" \
    "ds_read_b128 v[212:215], %[cur] offset:11264\n\t" \
    "v_exp_f32 v130, v130\n\t" \
    "v_exp_f32 v131, v131\n\t" \
    "v_exp_f32 v132, v132\n\t" \
    "v_mfma_f32_16x16x32_bf16 %[LY], %[ones], v[196:199], %[LY]\n\t" \
    "v_exp_f32 v133, v133\n\t" \
    "v_mfma_f32_32x32x16_bf16 v[144:159], v[200:203], %[qy0], v[176:191]\n\t" \
    "ds_read_b128 v[200:203], %[cur] offset:12288\n\t" \
    "v_exp_f32 v134, v134\n\t" \
    "v_exp_f32 v135, v135\n\t" \
    "v_cvt_pk_bf16_f32 v192, v128, v129\n\t" \
    "v_cvt_pk_bf16_f32 v193, v130, v131\n\t" \
    "v_mfma_f32_32x32x16_bf16 v[144:159], v[204:207], %[qy1], v[144:159]\n\t" \
    "ds_read_b128 v[204:207], %[cur] offset:13312\n\t" \
    "v_cvt_pk_bf16_f32 v194, v132, v133\n\t" \
    "v_cvt_pk_bf16_f32 v195, v134, v135\n\t" \
    "v_exp_f32 v136, v136\n\t" \
    "v_exp_f32 v137, v137\n\t" \
    "s_waitcnt lgkmcnt(3)\n\t" \
    "v_mfma_f32_32x32x16_bf16 %[OX], v[208:211], v[192:195], %[OX]\n\t" \
    "v_exp_f32 v138, v138\n\t" \
    "v_exp_f32 v139, v139\n\t" \
    "v_exp_f32 v140, v140\n\t" \
    "v_mfma_f32_16x16x32_bf16 %[LX], %[ones], v[192:195], %[LX]\n\t" \
    "v_exp_f32 v141, v141\n\t" \
    "v_exp_f32 v142, v142\n\t" \
    "v_exp_f32 v143, v143\n\t" \
    "v_cvt_pk_bf16_f32 v196, v136, v137\n\t" \
    "v_cvt_pk_bf16_f32 v197, v138, v139\n\t" \
    "v_cvt_pk_bf16_f32 v198, v140, v141\n\t" \
    "v_cvt_pk_bf16_f32 v199, v142, v143\n\t" \
    "v_exp_f32 v144, v144\n\t" \
    "v_exp_f32 v145, v145\n\t" \
    "s_waitcnt lgkmcnt(0)\n\t" \
    "v_mfma_f32_32x32x16_bf16 %[OX], v[212:215], v[196:199], %[OX]\n\t" \
    "v_exp_f32 v146, v146\n\t" \
    "v_exp_f32 v147, v147\n\t" \
    "v_exp_f32 v148, v148\n\t" \
    "v_mfma_f32_16x16x32_bf16 %[LX], %[ones], v[196:199], %[LX]\n\t" \
    "v_exp_f32 v149, v149\n\t" \
    "v_mfma_f32_32x32x16_bf16 v[128:143], v[200:203], %[qx0], v[160:175]\n\t" \
    "v_exp_f32 v150, v150\n\t" \
    "v_exp_f32 v151, v151\n\t" \
    "v_cvt_pk_bf16_f32 v192, v144, v145\n\t" \
    "v_cvt_pk_bf16_f32 v193, v146, v147\n\t" \
    "v_mfma_f32_32x32x16_bf16 v[128:143], v[204:207], %[qx1], v[128:143]\n\t" \
    "v_cvt_pk_bf16_f32 v194, v148, v149\n\t" \
    "v_cvt_pk_bf16_f32 v195, v150, v151\n\t" \
    "v_exp_f32 v152, v152\n\t" \
    "v_exp_f32 v153, v153\n\t" \
    "v_mfma_f32_32x32x16_bf16 %[OY], v[208:211], v[192:195], %[OY]\n\t" \
    "ds_read_b128 v[208:211], %[cur] offset:14336\n\t" \
    "v_exp_f32 v154, v154\n\t" \
    "v_exp_f32 v155, v155\n\t" \
    "v_exp_f32 v156, v156\n\t" \
    "v_mfma_f32_16x16x32_bf16 %[LY], %[ones], v[192:195], %[LY]\n\t" \
    "v_exp_f32 v157, v157\n\t" \
    "v_exp_f32 v158, v158\n\t" \
    "v_exp_f32 v159, v159\n\t" \
    "v_cvt_pk_bf16_f32 v196, v152, v153\n\t" \
    "v_cvt_pk_bf16_f32 v197, v154, v155\n\t" \
    "v_cvt_pk_bf16_f32 v198, v156, v157\n\t" \
    "v_cvt_pk_bf16_f32 v199, v158, v159\n\t" \
    "v_exp_f32 v128, v128\n\t" \
    "v_exp_f32 v129, v129\n\t" \
    "v_mfma_f32_32x32x16_bf16 %[OY], v[212:215], v[196:199], %[OY]\n\t" \
    "ds_read_b128 v[212:215], %[cur] offset:15360\n\t" \
    "v_exp_f32 v130, v130\n\t" \
    "v_exp_f32 v131, v131\n\t" \
    "v_exp_f32 v132, v132\n\t" \
    "v_mfma_f32_16x16x32_bf16 %[LY], %[ones], v[196:199], %[LY]\n\t" \
    "v_exp_f32 v133, v133\n\t" \
    "s_waitcnt vmcnt(12)\n\t" \
    "s_barrier\n\t" \
    "v_mfma_f32_32x32x16_bf16 v[144:159], v[200:203], %[qy0], v[176:191]\n\t" \
    "ds_read_b128 v[200:203], %[nxt] offset:0\n\t" \
    "v_exp_f32 v134, v134\n\t" \
    "v_exp_f32 v135, v135\n\t" \
    "v_cvt_pk_bf16_f32 v192, v128, v129\n\t" \
    "v_cvt_pk_bf16_f32 v193, v130, v131\n\t" \
    "v_mfma_f32_32x32x16_bf16 v[144:159], v[204:207], %[qy1], v[144:159]\n\t" \
    "ds_read_b128 v[204:207], %[nxt] offset:1024\n\t" \
    "v_cvt_pk_bf16_f32 v194, v132, v133\n\t" \
    "v_cvt_pk_bf16_f32 v195, v134, v135\n\t" \
    "v_exp_f32 v136, v136\n\t" \
    "v_exp_f32 v137, v137\n\t" \
    "s_waitcnt lgkmcnt(3)\n\t" \
    "v_mfma_f32_32x32x16_bf16 %[OX], v[208:211], v[192:195], %[OX]\n\t" \
    "v_exp_f32 v138, v138\n\t" \
    "v_exp_f32 v139, v139\n\t" \
    "v_exp_f32 v140, v140\n\t" \
    "v_mfma_f32_16x16x32_bf16 %[LX], %[ones], v[192:195], %[LX]\n\t" \
    "v_exp_f32 v141, v141\n\t" \
    "v_exp_f32 v142, v142\n\t" \
    "v_exp_f32 v143, v143\n\t" \
    "v_cvt_pk_bf16_f32 v196, v136, v137\n\t" \
    "v_cvt_pk_bf16_f32 v197, v138, v139\n\t" \
    "v_cvt_pk_bf16_f32 v198, v140, v141\n\t" \
    "v_cvt_pk_bf16_f32 v199, v142, v143\n\t" \
    "v_exp_f32 v144, v144\n\t" \
    "v_exp_f32 v145, v145\n\t" \
    "s_waitcnt lgkmcnt(0)\n\t" \
    "v_mfma_f32_32x32x16_bf16 %[OX], v[212:215], v[196:199], %[OX]\n\t" \
    "v_exp_f32 v146, v146\n\t" \
    "v_exp_f32 v147, v147\n\t" \
    "v_exp_f32 v148, v148\n\t" \
    "v_mfma_f32_16x16x32_bf16 %[LX], %[ones], v[196:199], %[LX]\n\t" \
    "v_exp_f32 v149, v149\n\t" \
    "v_mfma_f32_32x32x16_bf16 v[128:143], v[200:203], %[qx0], v[160:175]\n\t" \
    "v_exp_f32 v150, v150\n\t" \
    "v_exp_f32 v151, v151\n\t" \
    "v_cvt_pk_bf16_f32 v192, v144, v145\n\t" \
    "v_cvt_pk_bf16_f32 v193, v146, v147\n\t" \
    "v_mfma_f32_32x32x16_bf16 v[128:143], v[204:207], %[qx1], v[128:143]\n\t" \
    "v_cvt_pk_bf16_f32 v194, v148, v149\n\t" \
    "v_cvt_pk_bf16_f32 v195, v150, v151\n\t" \
    "v_exp_f32 v152, v152\n\t" \
    "v_exp_f32 v153, v153\n\t" \
    "v_mfma_f32_32x32x16_bf16 %[OY], v[208:211], v[192:195], %[OY]\n\t" \
    "ds_read_b128 v[208:211], %[nxt] offset:2048\n\t" \
    "v_exp_f32 v154, v154\n\t" \
    "v_exp_f32 v155, v155\n\t" \
    "v_exp_f32 v156, v156\n\t" \
    "v_mfma_f32_16x16x32_bf16 %[LY], %[ones], v[192:195], %[LY]\n\t" \
    "v_exp_f32 v157, v157\n\t" \
    "v_exp_f32 v158, v158\n\t" \
    "v_exp_f32 v159, v159\n\t" \
    "v_cvt_pk_bf16_f32 v196, v152, v153\n\t" \
    "v_cvt_pk_bf16_f32 v197, v154, v155\n\t" \
    "v_cvt_pk_bf16_f32 v198, v156, v157\n\t" \
    "v_cvt_pk_bf16_f32 v199, v158, v159\n\t" \
    "v_exp_f32 v128, v128\n\t" \
    "v_exp_f32 v129, v129\n\t" \
    "v_mov_b32 %[cur], %[nxt]\n\t" \
    "v_add_u32 %[nxt], %[sfar], %[vgrp]\n\t" \
    "s_waitcnt lgkmcnt(0)\n\t" \
    "v_mfma_f32_32x32x16_bf16 %[OY], v[212:215], v[196:199], %[OY]\n\t" \
    "v_mfma_f32_16x16x32_bf16 %[LY], %[ones], v[196:199], %[LY]\n\t" \
    "s_nop 15\n\t"
#define ATTN_BP_ASM \
    "s_nop 1\n\t" \
    "ds_read_b128 v[200:203], %[kv] offset:32768\n\t" \
    "ds_read_b128 v[204:207], %[kv] offset:33792\n\t" \
    "v_mov_b32 v218, 0xff800000\n\t" \
    "s_waitcnt lgkmcnt(0)\n\t" \
    "v_mfma_f32_32x32x16_bf16 v[128:143], v[200:203], %[q0], 0\n\t" \
    "v_mfma_f32_32x32x16_bf16 v[128:143], v[204:207], %[q1], v[128:143]\n\t" \
    "s_nop 15\n\t" \
    "v_max3_f32 v216, v128, v129, v130\n\t" \
    "v_max3_f32 v217, v131, v132, v133\n\t" \
    "v_max3_f32 v216, v216, v134, v135\n\t" \
    "v_max3_f32 v217, v217, v136, v137\n\t" \
    "v_max3_f32 v216, v216, v138, v139\n\t" \
    "v_max3_f32 v217, v217, v140, v141\n\t" \
    "v_max3_f32 v216, v216, v142, v143\n\t" \
    "v_max_f32 v216, v216, v217\n\t" \
    "v_mov_b32 v217, v216\n\t" \
    "s_nop 1\n\t" \
    "v_permlane32_swap_b32 v216, v217\n\t" \
    "s_nop 1\n\t" \
    "v_max_f32 v216, v216, v217\n\t" \
    "v_sub_f32 v217, 0, v216\n\t" \
    "s_mov_b32 s40, 0xffffffff\n\t" \
    "s_mov_b32 s41, 0xfffffff0\n\t" \
    "v_sub_f32 v128, v128, v216\n\t" \
    "v_mov_b32 v160, v217\n\t" \
    "v_cndmask_b32_e64 v176, v217, v218, s[40:41]\n\t" \
    "v_cndmask_b32_e64 v128, v218, v128, s[40:41]\n\t" \
    "s_mov_b32 s40, 0xfffffffe\n\t" \
    "s_mov_b32 s41, 0xffffffe0\n\t" \
    "v_sub_f32 v129, v129, v216\n\t" \
    "v_mov_b32 v161, v217\n\t" \
    "v_cndmask_b32_e64 v177, v217, v218, s[40:41]\n\t" \
    "v_cndmask_b32_e64 v129, v218, v129, s[40:41]\n\t" \
    "s_mov_b32 s40, 0xfffffffc\n\t" \
    "s_mov_b32 s41, 0xffffffc0\n\t" \
    "v_sub_f32 v130, v130, v216\n\t" \
    "v_mov_b32 v162, v217\n\t" \
    "v_cndmask_b32_e64 v178, v217, v218, s[40:41]\n\t" \
    "v_cndmask_b32_e64 v130, v218, v130, s[40:41]\n\t" \
    "s_mov_b32 s40, 0xfffffff8\n\t" \
    "s_mov_b32 s41, 0xffffff80\n\t" \
    "v_sub_f32 v131, v131, v216\n\t" \
    "v_mov_b32 v163, v217\n\t" \
    "v_cndmask_b32_e64 v179, v217, v218, s[40:41]\n\t" \
    "v_cndmask_b32_e64 v131, v218, v131, s[40:41]\n\t" \
    "s_mov_b32 s40, 0xffffff00\n\t" \
    "s_mov_b32 s41, 0xfffff000\n\t" \
    "v_sub_f32 v132, v132, v216\n\t" \
    "v_mov_b32 v164, v217\n\t" \
    "v_cndmask_b32_e64 v180, v217, v218, s[40:41]\n\t" \
    "v_cndmask_b32_e64 v132, v218, v132, s[40:41]\n\t" \
    "s_mov_b32 s40, 0xfffffe00\n\t" \
    "s_mov_b32 s41, 0xffffe000\n\t" \
    "v_sub_f32 v133, v133, v216\n\t" \
    "v_mov_b32 v165, v217\n\t" \
    "v_cndmask_b32_e64 v181, v217, v218, s[40:41]\n\t" \
    "v_cndmask_b32_e64 v133, v218, v133, s[40:41]\n\t" \
    "s_mov_b32 s40, 0xfffffc00\n\t" \
    "s_mov_b32 s41, 0xffffc000\n\t" \
    "v_sub_f32 v134, v134, v216\n\t" \
    "v_mov_b32 v166, v217\n\t" \
    "v_cndmask_b32_e64 v182, v217, v218, s[40:41]\n\t" \
    "v_cndmask_b32_e64 v134, v218, v134, s[40:41]\n\t" \
    "s_mov_b32 s40, 0xfffff800\n\t" \
    "s_mov_b32 s41, 0xffff8000\n\t" \
    "v_sub_f32 v135, v135, v216\n\t" \
    "v_mov_b32 v167, v217\n\t" \
    "v_cndmask_b32_e64 v183, v217, v218, s[40:41]\n\t" \
    "v_cndmask_b32_e64 v135, v218, v135, s[40:41]\n\t" \
    "s_mov_b32 s40, 0xffff0000\n\t" \
    "s_mov_b32 s41, 0xfff00000\n\t" \
    "v_sub_f32 v136, v136, v216\n\t" \
    "v_mov_b32 v168, v217\n\t" \
    "v_cndmask_b32_e64 v184, v217, v218, s[40:41]\n\t" \
    "v_cndmask_b32_e64 v136, v218, v136, s[40:41]\n\t" \
    "s_mov_b32 s40, 0xfffe0000\n\t" \
    "s_mov_b32 s41, 0xffe00000\n\t" \
    "v_sub_f32 v137, v137, v216\n\t" \
    "v_mov_b32 v169, v217\n\t" \
    "v_cndmask_b32_e64 v185, v217, v218, s[40:41]\n\t" \
    "v_cndmask_b32_e64 v137, v218, v137, s[40:41]\n\t" \
    "s_mov_b32 s40, 0xfffc0000\n\t" \
    "s_mov_b32 s41, 0xffc00000\n\t" \
    "v_sub_f32 v138, v138, v216\n\t" \
    "v_mov_b32 v170, v217\n\t" \
    "v_cndmask_b32_e64 v186, v217, v218, s[40:41]\n\t" \
    "v_cndmask_b32_e64 v138, v218, v138, s[40:41]\n\t" \
    "s_mov_b32 s40, 0xfff80000\n\t" \
    "s_mov_b32 s41, 0xff800000\n\t" \
    "v_sub_f32 v139, v139, v216\n\t" \
    "v_mov_b32 v171, v217\n\t" \
    "v_cndmask_b32_e64 v187, v217, v218, s[40:41]\n\t" \
    "v_cndmask_b32_e64 v139, v218, v139, s[40:41]\n\t" \
    "s_mov_b32 s40, 0xff000000\n\t" \
    "s_mov_b32 s41, 0xf0000000\n\t" \
    "v_sub_f32 v140, v140, v216\n\t" \
    "v_mov_b32 v172, v217\n\t" \
    "v_cndmask_b32_e64 v188, v217, v218, s[40:41]\n\t" \
    "v_cndmask_b32_e64 v140, v218, v140, s[40:41]\n\t" \
    "s_mov_b32 s40, 0xfe000000\n\t" \
    "s_mov_b32 s41, 0xe0000000\n\t" \
    "v_sub_f32 v141, v141, v216\n\t" \
    "v_mov_b32 v173, v217\n\t" \
    "v_cndmask_b32_e64 v189, v217, v218, s[40:41]\n\t" \
    "v_cndmask_b32_e64 v141, v218, v141, s[40:41]\n\t" \
    "s_mov_b32 s40, 0xfc000000\n\t" \
    "s_mov_b32 s41, 0xc0000000\n\t" \
    "v_sub_f32 v142, v142, v216\n\t" \
    "v_mov_b32 v174, v217\n\t" \
    "v_cndmask_b32_e64 v190, v217, v218, s[40:41]\n\t" \
    "v_cndmask_b32_e64 v142, v218, v142, s[40:41]\n\t" \
    "s_mov_b32 s40, 0xf8000000\n\t" \
    "s_mov_b32 s41, 0x80000000\n\t" \
    "v_sub_f32 v143, v143, v216\n\t" \
    "v_mov_b32 v175, v217\n\t" \
    "v_cndmask_b32_e64 v191, v217, v218, s[40:41]\n\t" \
    "v_cndmask_b32_e64 v143, v218, v143, s[40:41]\n\t" \
    "v_exp_f32 v128, v128\n\t" \
    "v_exp_f32 v129, v129\n\t" \
    "s_nop 1\n\t"
#define ATTN_B_ASM \
    "s_nop 1\n\t" \
    "ds_read_b128 v[200:203], %[kv] offset:28672\n\t" \
    "ds_read_b128 v[204:207], %[kv] offset:29696\n\t" \
    "ds_read_b128 v[208:211], %[kv] offset:34816\n\t" \
    "v_mov_b32 v196, 0\n\t" \
    "v_mov_b32 v197, 0\n\t" \
    "v_mov_b32 v198, 0\n\t" \
    "v_mov_b32 v199, 0\n\t" \
    "v_mov_b32 v212, 0\n\t" \
    "v_mov_b32 v213, 0\n\t" \
    "v_mov_b32 v214, 0\n\t" \
    "v_mov_b32 v215, 0\n\t" \
    "s_waitcnt lgkmcnt(0)\n\t" \
    "s_waitcnt lgkmcnt(3)\n\t" \
    "v_mfma_f32_32x32x16_bf16 %[O], v[212:215], v[196:199], %[O]\n\t" \
    "ds_read_b128 v[212:215], %[kv] offset:35840\n\t" \
    "v_exp_f32 v130, v130\n\t" \
    "v_exp_f32 v131, v131\n\t" \
    "v_exp_f32 v132, v132\n\t" \
    "v_mfma_f32_16x16x32_bf16 %[L], %[ones], v[196:199], %[L]\n\t" \
    "v_exp_f32 v133, v133\n\t" \
    "s_waitcnt lgkmcnt(3)\n\t" \
    "v_mfma_f32_32x32x16_bf16 v[144:159], v[200:203], %[q0], v[160:175]\n\t" \
    "ds_read_b128 v[200:203], %[kv] offset:24576\n\t" \
    "v_exp_f32 v134, v134\n\t" \
    "v_exp_f32 v135, v135\n\t" \
    "v_cvt_pk_bf16_f32 v192, v128, v129\n\t" \
    "v_cvt_pk_bf16_f32 v193, v130, v131\n\t" \
    "s_waitcnt lgkmcnt(3)\n\t" \
    "v_mfma_f32_32x32x16_bf16 v[144:159], v[204:207], %[q1], v[144:159]\n\t" \
    "ds_read_b128 v[204:207], %[kv] offset:25600\n\t" \
    "v_cvt_pk_bf16_f32 v194, v132, v133\n\t" \
    "v_cvt_pk_bf16_f32 v195, v134, v135\n\t" \
    "v_exp_f32 v136, v136\n\t" \
    "v_exp_f32 v137, v137\n\t" \
    "s_waitcnt lgkmcnt(3)\n\t" \
    "v_mfma_f32_32x32x16_bf16 %[O], v[208:211], v[192:195], %[O]\n\t" \
    "ds_read_b128 v[208:211], %[kv] offset:30720\n\t" \
    "v_exp_f32 v138, v138\n\t" \
    "v_exp_f32 v139, v139\n\t" \
    "v_exp_f32 v140, v140\n\t" \
    "v_mfma_f32_16x16x32_bf16 %[L], %[ones], v[192:195], %[L]\n\t" \
    "v_exp_f32 v141, v141\n\t" \
    "v_exp_f32 v142, v142\n\t" \
    "v_exp_f32 v143, v143\n\t" \
    "v_cvt_pk_bf16_f32 v196, v136, v137\n\t" \
    "v_cvt_pk_bf16_f32 v197, v138, v139\n\t" \
    "v_cvt_pk_bf16_f32 v198, v140, v141\n\t" \
    "v_cvt_pk_bf16_f32 v199, v142, v143\n\t" \
    "v_exp_f32 v144, v144\n\t" \
    "v_exp_f32 v145, v145\n\t" \
    "s_cmp_eq_u32 %[nt], 1\n\t" \
    "s_cbranch_scc1 3f\n\t" \
    "s_waitcnt lgkmcnt(3)\n\t" \
    "v_mfma_f32_32x32x16_bf16 %[O], v[212:215], v[196:199], %[O]\n\t" \
    "ds_read_b128 v[212:215], %[kv] offset:31744\n\t" \
    "v_exp_f32 v146, v146\n\t" \
    "v_exp_f32 v147, v147\n\t" \
    "v_exp_f32 v148, v148\n\t" \
    "v_mfma_f32_16x16x32_bf16 %[L], %[ones], v[196:199], %[L]\n\t" \
    "v_exp_f32 v149, v149\n\t" \
    "s_waitcnt lgkmcnt(3)\n\t" \
    "v_mfma_f32_32x32x16_bf16 v[128:143], v[200:203], %[q0], v[160:175]\n\t" \
    "ds_read_b128 v[200:203], %[kv] offset:20480\n\t" \
    "v_exp_f32 v150, v150\n\t" \
    "v_exp_f32 v151, v151\n\t" \
    "v_cvt_pk_bf16_f32 v192, v144, v145\n\t" \
    "v_cvt_pk_bf16_f32 v193, v146, v147\n\t" \
    "s_waitcnt lgkmcnt(3)\n\t" \
    "v_mfma_f32_32x32x16_bf16 v[128:143], v[204:207], %[q1], v[128:143]\n\t" \
    "ds_read_b128 v[204:207], %[kv] offset:21504\n\t" \
    "v_cvt_pk_bf16_f32 v194, v148, v149\n\t" \
    "v_cvt_pk_bf16_f32 v195, v150, v151\n\t" \
    "v_exp_f32 v152, v152\n\t" \
    "v_exp_f32 v153, v153\n\t" \
    "s_waitcnt lgkmcnt(3)\n\t" \
    "v_mfma_f32_32x32x16_bf16 %[O], v[208:211], v[192:195], %[O]\n\t" \
    "ds_read_b128 v[208:211], %[kv] offset:26624\n\t" \
    "v_exp_f32 v154, v154\n\t" \
    "v_exp_f32 v155, v155\n\t" \
    "v_exp_f32 v156, v156\n\t" \
    "v_mfma_f32_16x16x32_bf16 %[L], %[ones], v[192:195], %[L]\n\t" \
    "v_exp_f32 v157, v157\n\t" \
    "v_exp_f32 v158, v158\n\t" \
    "v_exp_f32 v159, v159\n\t" \
    "v_cvt_pk_bf16_f32 v196, v152, v153\n\t" \
    "v_cvt_pk_bf16_f32 v197, v154, v155\n\t" \
    "v_cvt_pk_bf16_f32 v198, v156, v157\n\t" \
    "v_cvt_pk_bf16_f32 v199, v158, v159\n\t" \
    "v_exp_f32 v128, v128\n\t" \
    "v_exp_f32 v129, v129\n\t" \
    "s_cmp_eq_u32 %[nt], 2\n\t" \
    "s_cbranch_scc1 3f\n\t" \
    "s_waitcnt lgkmcnt(3)\n\t" \
    "v_mfma_f32_32x32x16_bf16 %[O], v[212:215], v[196:199], %[O]\n\t" \
    "ds_read_b128 v[212:215], %[kv] offset:27648\n\t" \
    "v_exp_f32 v130, v130\n\t" \
    "v_exp_f32 v131, v131\n\t" \
    "v_exp_f32 v132, v132\n\t" \
    "v_mfma_f32_16x16x32_bf16 %[L], %[ones], v[196:199], %[L]\n\t" \
    "v_exp_f32 v133, v133\n\t" \
    "s_waitcnt lgkmcnt(3)\n\t" \
    "v_mfma_f32_32x32x16_bf16 v[144:159], v[200:203], %[q0], v[160:175]\n\t" \
    "ds_read_b128 v[200:203], %[kv] offset:16384\n\t" \
    "v_exp_f32 v134, v134\n\t" \
    "v_exp_f32 v135, v135\n\t" \
    "v_cvt_pk_bf16_f32 v192, v128, v129\n\t" \
    "v_cvt_pk_bf16_f32 v193, v130, v131\n\t" \
    "s_waitcnt lgkmcnt(3)\n\t" \
    "v_mfma_f32_32x32x16_bf16 v[144:159], v[204:207], %[q1], v[144:159]\n\t" \
    "ds_read_b128 v[204:207], %[kv] offset:17408\n\t" \
    "v_cvt_pk_bf16_f32 v194, v132, v133\n\t" \
    "v_cvt_pk_bf16_f32 v195, v134, v135\n\t" \
    "v_exp_f32 v136, v136\n\t" \
    "v_exp_f32 v137, v137\n\t" \
    "s_waitcnt lgkmcnt(3)\n\t" \
    "v_mfma_f32_32x32x16_bf16 %[O], v[208:211], v[192:195], %[O]\n\t" \
    "ds_read_b128 v[208:211], %[kv] offset:22528\n\t" \
    "v_exp_f32 v138, v138\n\t" \
    "v_exp_f32 v139, v139\n\t" \
    "v_exp_f32 v140, v140\n\t" \
    "v_mfma_f32_16x16x32_bf16 %[L], %[ones], v[192:195], %[L]\n\t" \
    "v_exp_f32 v141, v141\n\t" \
    "v_exp_f32 v142, v142\n\t" \
    "v_exp_f32 v143, v143\n\t" \
    "v_cvt_pk_bf16_f32 v196, v136, v137\n\t" \
    "v_cvt_pk_bf16_f32 v197, v138, v139\n\t" \
    "v_cvt_pk_bf16_f32 v198, v140, v141\n\t" \
    "v_cvt_pk_bf16_f32 v199, v142, v143\n\t" \
    "v_exp_f32 v144, v144\n\t" \
    "v_exp_f32 v145, v145\n\t" \
    "s_cmp_eq_u32 %[nt], 3\n\t" \
    "s_cbranch_scc1 3f\n\t" \
    "s_waitcnt lgkmcnt(3)\n\t" \
    "v_mfma_f32_32x32x16_bf16 %[O], v[212:215], v[196:199], %[O]\n\t" \
    "ds_read_b128 v[212:215], %[kv] offset:23552\n\t" \
    "v_exp_f32 v146, v146\n\t" \
    "v_exp_f32 v147, v147\n\t" \
    "v_exp_f32 v148, v148\n\t" \
    "v_mfma_f32_16x16x32_bf16 %[L], %[ones], v[196:199], %[L]\n\t" \
    "v_exp_f32 v149, v149\n\t" \
    "s_waitcnt lgkmcnt(3)\n\t" \
    "v_mfma_f32_32x32x16_bf16 v[128:143], v[200:203], %[q0], v[160:175]\n\t" \
    "ds_read_b128 v[200:203], %[kv] offset:12288\n\t" \
    "v_exp_f32 v150, v150\n\t" \
    "v_exp_f32 v151, v151\n\t" \
    "v_cvt_pk_bf16_f32 v192, v144, v145\n\t" \
    "v_cvt_pk_bf16_f32 v193, v146, v147\n\t" \
    "s_waitcnt lgkmcnt(3)\n\t" \
    "v_mfma_f32_32x32x16_bf16 v[128:143], v[204:207], %[q1], v[128:143]\n\t" \
    "ds_read_b128 v[204:207], %[kv] offset:13312\n\t" \
    "v_cvt_pk_bf16_f32 v194, v148, v149\n\t" \
    "v_cvt_pk_bf16_f32 v195, v150, v151\n\t" \
    "v_exp_f32 v152, v152\n\t" \
    "v_exp_f32 v153, v153\n\t" \
    "s_waitcnt lgkmcnt(3)\n\t" \
    "v_mfma_f32_32x32x16_bf16 %[O], v[208:211], v[192:195], %[O]\n\t" \
    "ds_read_b128 v[208:211], %[kv] offset:18432\n\t" \
    "v_exp_f32 v154, v154\n\t" \
    "v_exp_f32 v155, v155\n\t" \
    "v_exp_f32 v156, v156\n\t" \
    "v_mfma_f32_16x16x32_bf16 %[L], %[ones], v[192:195], %[L]\n\t" \
    "v_exp_f32 v157, v157\n\t" \
    "v_exp_f32 v158, v158\n\t" \
    "v_exp_f32 v159, v159\n\t" \
    "v_cvt_pk_bf16_f32 v196, v152, v153\n\t" \
    "v_cvt_pk_bf16_f32 v197, v154, v155\n\t" \
    "v_cvt_pk_bf16_f32 v198, v156, v157\n\t" \
    "v_cvt_pk_bf16_f32 v199, v158, v159\n\t" \
    "v_exp_f32 v128, v128\n\t" \
    "v_exp_f32 v129, v129\n\t" \
    "s_cmp_eq_u32 %[nt], 4\n\t" \
    "s_cbranch_scc1 3f\n\t" \
    "s_waitcnt lgkmcnt(3)\n\t" \
    "v_mfma_f32_32x32x16_bf16 %[O], v[212:215], v[196:199], %[O]\n\t" \
    "ds_read_b128 v[212:215], %[kv] offset:19456\n\t" \
    "v_exp_f32 v130, v130\n\t" \
    "v_exp_f32 v131, v131\n\t" \
    "v_exp_f32 v132, v132\n\t" \
    "v_mfma_f32_16x16x32_bf16 %[L], %[ones], v[196:199], %[L]\n\t" \
    "v_exp_f32 v133, v133\n\t" \
    "s_waitcnt vmcnt(0)\n\t" \
    "s_barrier\n\t" \
    "s_waitcnt lgkmcnt(3)\n\t" \
    "v_mfma_f32_32x32x16_bf16 v[144:159], v[200:203], %[q0], v[160:175]\n\t" \
    "ds_read_b128 v[200:203], %[kv] offset:8192\n\t" \
    "v_exp_f32 v134, v134\n\t" \
    "v_exp_f32 v135, v135\n\t" \
    "v_cvt_pk_bf16_f32 v192, v128, v129\n\t" \
    "v_cvt_pk_bf16_f32 v193, v130, v131\n\t" \
    "s_waitcnt lgkmcnt(3)\n\t" \
    "v_mfma_f32_32x32x16_bf16 v[144:159], v[204:207], %[q1], v[144:159]\n\t" \
    "ds_read_b128 v[204:207], %[kv] offset:9216\n\t" \
    "v_cvt_pk_bf16_f32 v194, v132, v133\n\t" \
    "v_cvt_pk_bf16_f32 v195, v134, v135\n\t" \
    "v_exp_f32 v136, v136\n\t" \
    "v_exp_f32 v137, v137\n\t" \
    "s_waitcnt lgkmcnt(3)\n\t" \
    "v_mfma_f32_32x32x16_bf16 %[O], v[208:211], v[192:195], %[O]\n\t" \
    "ds_read_b128 v[208:211], %[kv] offset:14336\n\t" \
    "v_exp_f32 v138, v138\n\t" \
    "v_exp_f32 v139, v139\n\t" \
    "v_exp_f32 v140, v140\n\t" \
    "v_mfma_f32_16x16x32_bf16 %[L], %[ones], v[192:195], %[L]\n\t" \
    "v_exp_f32 v141, v141\n\t" \
    "v_exp_f32 v142, v142\n\t" \
    "v_exp_f32 v143, v143\n\t" \
    "v_cvt_pk_bf16_f32 v196, v136, v137\n\t" \
    "v_cvt_pk_bf16_f32 v197, v138, v139\n\t" \
    "v_cvt_pk_bf16_f32 v198, v140, v141\n\t" \
    "v_cvt_pk_bf16_f32 v199, v142, v143\n\t" \
    "v_exp_f32 v144, v144\n\t" \
    "v_exp_f32 v145, v145\n\t" \
    "s_cmp_eq_u32 %[nt], 5\n\t" \
    "s_cbranch_scc1 3f\n\t" \
    "s_waitcnt lgkmcnt(3)\n\t" \
    "v_mfma_f32_32x32x16_bf16 %[O], v[212:215], v[196:199], %[O]\n\t" \
    "ds_read_b128 v[212:215], %[kv] offset:15360\n\t" \
    "v_exp_f32 v146, v146\n\t" \
    "v_exp_f32 v147, v147\n\t" \
    "v_exp_f32 v148, v148\n\t" \
    "v_mfma_f32_16x16x32_bf16 %[L], %[ones], v[196:199], %[L]\n\t" \
    "v_exp_f32 v149, v149\n\t" \
    "s_waitcnt lgkmcnt(3)\n\t" \
    "v_mfma_f32_32x32x16_bf16 v[128:143], v[200:203], %[q0], v[160:175]\n\t" \
    "ds_read_b128 v[200:203], %[kv] offset:4096\n\t" \
    "v_exp_f32 v150, v150\n\t" \
    "v_exp_f32 v151, v151\n\t" \
    "v_cvt_pk_bf16_f32 v192, v144, v145\n\t" \
    "v_cvt_pk_bf16_f32 v193, v146, v147\n\t" \
    "s_waitcnt lgkmcnt(3)\n\t" \
    "v_mfma_f32_32x32x16_bf16 v[128:143], v[204:207], %[q1], v[128:143]\n\t" \
    "ds_read_b128 v[204:207], %[kv] offset:5120\n\t" \
    "v_cvt_pk_bf16_f32 v194, v148, v149\n\t" \
    "v_cvt_pk_bf16_f32 v195, v150, v151\n\t" \
    "v_exp_f32 v152, v152\n\t" \
    "v_exp_f32 v153, v153\n\t" \
    "s_waitcnt lgkmcnt(3)\n\t" \
    "v_mfma_f32_32x32x16_bf16 %[O], v[208:211], v[192:195], %[O]\n\t" \
    "ds_read_b128 v[208:211], %[kv] offset:10240\n\t" \
    "v_exp_f32 v154, v154\n\t" \
    "v_exp_f32 v155, v155\n\t" \
    "v_exp_f32 v156, v156\n\t" \
    "v_mfma_f32_16x16x32_bf16 %[L], %[ones], v[192:195], %[L]\n\t" \
    "v_exp_f32 v157, v157\n\t" \
    "v_exp_f32 v158, v158\n\t" \
    "v_exp_f32 v159, v159\n\t" \
    "v_cvt_pk_bf16_f32 v196, v152, v153\n\t" \
    "v_cvt_pk_bf16_f32 v197, v154, v155\n\t" \
    "v_cvt_pk_bf16_f32 v198, v156, v157\n\t" \
    "v_cvt_pk_bf16_f32 v199, v158, v159\n\t" \
    "v_exp_f32 v128, v128\n\t" \
    "v_exp_f32 v129, v129\n\t" \
    "s_cmp_eq_u32 %[nt], 6\n\t" \
    "s_cbranch_scc1 3f\n\t" \
    "s_waitcnt lgkmcnt(3)\n\t" \
    "v_mfma_f32_32x32x16_bf16 %[O], v[212:215], v[196:199], %[O]\n\t" \
    "ds_read_b128 v[212:215], %[kv] offset:11264\n\t" \
    "v_exp_f32 v130, v130\n\t" \
    "v_exp_f32 v131, v131\n\t" \
    "v_exp_f32 v132, v132\n\t" \
    "v_mfma_f32_16x16x32_bf16 %[L], %[ones], v[196:199], %[L]\n\t" \
    "v_exp_f32 v133, v133\n\t" \
    "s_waitcnt lgkmcnt(3)\n\t" \
    "v_mfma_f32_32x32x16_bf16 v[144:159], v[200:203], %[q0], v[160:175]\n\t" \
    "ds_read_b128 v[200:203], %[kv] offset:0\n\t" \
    "v_exp_f32 v134, v134\n\t" \
    "v_exp_f32 v135, v135\n\t" \
    "v_cvt_pk_bf16_f32 v192, v128, v129\n\t" \
    "v_cvt_pk_bf16_f32 v193, v130, v131\n\t" \
    "s_waitcnt lgkmcnt(3)\n\t" \
    "v_mfma_f32_32x32x16_bf16 v[144:159], v[204:207], %[q1], v[144:159]\n\t" \
    "ds_read_b128 v[204:207], %[kv] offset:1024\n\t" \
    "v_cvt_pk_bf16_f32 v194, v132, v133\n\t" \
    "v_cvt_pk_bf16_f32 v195, v134, v135\n\t" \
    "v_exp_f32 v136, v136\n\t" \
    "v_exp_f32 v137, v137\n\t" \
    "s_waitcnt lgkmcnt(3)\n\t" \
    "v_mfma_f32_32x32x16_bf16 %[O], v[208:211], v[192:195], %[O]\n\t" \
    "ds_read_b128 v[208:211], %[kv] offset:6144\n\t" \
    "v_exp_f32 v138, v138\n\t" \
    "v_exp_f32 v139, v139\n\t" \
    "v_exp_f32 v140, v140\n\t" \
    "v_mfma_f32_16x16x32_bf16 %[L], %[ones], v[192:195], %[L]\n\t" \
    "v_exp_f32 v141, v141\n\t" \
    "v_exp_f32 v142, v142\n\t" \
    "v_exp_f32 v143, v143\n\t" \
    "v_cvt_pk_bf16_f32 v196, v136, v137\n\t" \
    "v_cvt_pk_bf16_f32 v197, v138, v139\n\t" \
    "v_cvt_pk_bf16_f32 v198, v140, v141\n\t" \
    "v_cvt_pk_bf16_f32 v199, v142, v143\n\t" \
    "v_exp_f32 v144, v144\n\t" \
    "v_exp_f32 v145, v145\n\t" \
    "s_cmp_eq_u32 %[nt], 7\n\t" \
    "s_cbranch_scc1 3f\n\t" \
    "s_waitcnt lgkmcnt(3)\n\t" \
    "v_mfma_f32_32x32x16_bf16 %[O], v[212:215], v[196:199], %[O]\n\t" \
    "ds_read_b128 v[212:215], %[kv] offset:7168\n\t" \
    "v_exp_f32 v146, v146\n\t" \
    "v_exp_f32 v147, v147\n\t" \
    "v_exp_f32 v148, v148\n\t" \
    "v_mfma_f32_16x16x32_bf16 %[L], %[ones], v[196:199], %[L]\n\t" \
    "v_exp_f32 v149, v149\n\t" \
    "s_waitcnt lgkmcnt(3)\n\t" \
    "v_mfma_f32_32x32x16_bf16 v[128:143], v[200:203], %[q0], v[176:191]\n\t" \
    "ds_read_b128 v[200:203], %[kv] offset:0\n\t" \
    "v_exp_f32 v150, v150\n\t" \
    "v_exp_f32 v151, v151\n\t" \
    "v_cvt_pk_bf16_f32 v192, v144, v145\n\t" \
    "v_cvt_pk_bf16_f32 v193, v146, v147\n\t" \
    "s_waitcnt lgkmcnt(3)\n\t" \
    "v_mfma_f32_32x32x16_bf16 v[128:143], v[204:207], %[q1], v[128:143]\n\t" \
    "ds_read_b128 v[204:207], %[kv] offset:1024\n\t" \
    "v_cvt_pk_bf16_f32 v194, v148, v149\n\t" \
    "v_cvt_pk_bf16_f32 v195, v150, v151\n\t" \
    "v_exp_f32 v152, v152\n\t" \
    "v_exp_f32 v153, v153\n\t" \
    "s_waitcnt lgkmcnt(3)\n\t" \
    "v_mfma_f32_32x32x16_bf16 %[O], v[208:211], v[192:195], %[O]\n\t" \
    "ds_read_b128 v[208:211], %[kv] offset:2048\n\t" \
    "v_exp_f32 v154, v154\n\t" \
    "v_exp_f32 v155, v155\n\t" \
    "v_exp_f32 v156, v156\n\t" \
    "v_mfma_f32_16x16x32_bf16 %[L], %[ones], v[192:195], %[L]\n\t" \
    "v_exp_f32 v157, v157\n\t" \
    "v_exp_f32 v158, v158\n\t" \
    "v_exp_f32 v159, v159\n\t" \
    "v_cvt_pk_bf16_f32 v196, v152, v153\n\t" \
    "v_cvt_pk_bf16_f32 v197, v154, v155\n\t" \
    "v_cvt_pk_bf16_f32 v198, v156, v157\n\t" \
    "v_cvt_pk_bf16_f32 v199, v158, v159\n\t" \
    "v_exp_f32 v128, v128\n\t" \
    "v_exp_f32 v129, v129\n\t" \
    "s_cmp_eq_u32 %[nt], 8\n\t" \
    "s_cbranch_scc1 3f\n\t" \
    "s_waitcnt lgkmcnt(3)\n\t" \
    "v_mfma_f32_32x32x16_bf16 %[O], v[212:215], v[196:199], %[O]\n\t" \
    "ds_read_b128 v[212:215], %[kv] offset:3072\n\t" \
    "v_exp_f32 v130, v130\n\t" \
    "v_exp_f32 v131, v131\n\t" \
    "v_exp_f32 v132, v132\n\t" \
    "v_mfma_f32_16x16x32_bf16 %[L], %[ones], v[196:199], %[L]\n\t" \
    "v_exp_f32 v133, v133\n\t" \
    "s_waitcnt lgkmcnt(3)\n\t" \
    "v_mfma_f32_32x32x16_bf16 v[144:159], v[200:203], %[q0], v[160:175]\n\t" \
    "ds_read_b128 v[200:203], %[kv] offset:0\n\t" \
    "v_exp_f32 v134, v134\n\t" \
    "v_exp_f32 v135, v135\n\t" \
    "v_cvt_pk_bf16_f32 v192, v128, v129\n\t" \
    "v_cvt_pk_bf16_f32 v193, v130, v131\n\t" \
    "s_waitcnt lgkmcnt(3)\n\t" \
    "v_mfma_f32_32x32x16_bf16 v[144:159], v[204:207], %[q1], v[144:159]\n\t" \
    "ds_read_b128 v[204:207], %[kv] offset:1024\n\t" \
    "v_cvt_pk_bf16_f32 v194, v132, v133\n\t" \
    "v_cvt_pk_bf16_f32 v195, v134, v135\n\t" \
    "v_exp_f32 v136, v136\n\t" \
    "v_exp_f32 v137, v137\n\t" \
    "s_waitcnt lgkmcnt(3)\n\t" \
    "v_mfma_f32_32x32x16_bf16 %[O], v[208:211], v[192:195], %[O]\n\t" \
    "ds_read_b128 v[208:211], %[kv] offset:2048\n\t" \
    "v_exp_f32 v138, v138\n\t" \
    "v_exp_f32 v139, v139\n\t" \
    "v_exp_f32 v140, v140\n\t" \
    "v_mfma_f32_16x16x32_bf16 %[L], %[ones], v[192:195], %[L]\n\t" \
    "v_exp_f32 v141, v141\n\t" \
    "v_exp_f32 v142, v142\n\t" \
    "v_exp_f32 v143, v143\n\t" \
    "v_cvt_pk_bf16_f32 v196, v136, v137\n\t" \
    "v_cvt_pk_bf16_f32 v197, v138, v139\n\t" \
    "v_cvt_pk_bf16_f32 v198, v140, v141\n\t" \
    "v_cvt_pk_bf16_f32 v199, v142, v143\n\t" \
    "v_exp_f32 v144, v144\n\t" \
    "v_exp_f32 v145, v145\n\t" \
    "3:\n\t" \
    "s_cmp_gt_u32 %[nt], 4\n\t" \
    "s_cbranch_scc1 4f\n\t" \
    "s_waitcnt vmcnt(0)\n\t" \
    "s_barrier\n\t" \
    "4:\n\t" \
    "s_waitcnt lgkmcnt(0)\n\t" \
    "v_mfma_f32_32x32x16_bf16 %[O], v[212:215], v[196:199], %[O]\n\t" \
    "v_mfma_f32_16x16x32_bf16 %[L], %[ones], v[196:199], %[L]\n\t" \
    "s_nop 15\n\t"
#define ATTN_CLOBBERS "v128", "v129", "v130", "v131", "v132", "v133", "v134", "v135", "v136", "v137", "v138", "v139", "v140", "v141", "v142", "v143", "v144", "v145", "v146", "v147", "v148", "v149", "v150", "v151", "v152", "v153", "v154", "v155", "v156", "v157", "v158", "v159", "v160", "v161", "v162", "v163", "v164", "v165", "v166", "v167", "v168", "v169", "v170", "v171", "v172", "v173", "v174", "v175", "v176", "v177", "v178", "v179", "v180", "v181", "v182", "v183", "v184", "v185", "v186", "v187", "v188", "v189", "v190", "v191", "v192", "v193", "v194", "v195", "v196", "v197", "v198", "v199", "v200", "v201", "v202", "v203", "v204", "v205", "v206", "v207", "v208", "v209", "v210", "v211", "v212", "v213", "v214", "v215", "v216", "v217", "v218", "v219", "v220", "v221", "v222", "v223", "s40", "s41", "m0", "scc", "vcc", "memory"
#define ATTN_B_CLOBBERS "v144", "v145", "v146", "v147", "v148", "v149", "v150", "v151", "v152", "v153", "v154", "v155", "v156", "v157", "v158", "v159", "v192", "v193", "v194", "v195", "v196", "v197", "v198", "v199", "v200", "v201", "v202", "v203", "v204", "v205", "v206", "v207", "v208", "v209", "v210", "v211", "v212", "v213", "v214", "v215", "v216", "v217", "v218", "v219", "v220", "v221", "v222", "v223", "s40", "s41", "m0", "scc", "vcc", "memory"
#define PROJ_ASM_QK \
    "s_nop 1\n\t" \
    "v_xor_b32 v204, 64, %[xa]\n\t" \
    "v_xor_b32 v205, 32, %[wo]\n\t" \
    "v_xor_b32 v206, 64, %[wo]\n\t" \
    "v_xor_b32 v207, 0x60, %[wo]\n\t" \
    "ds_read_b64_tr_b16 v[160:161], %[wo] offset:0\n\t" \
    "ds_read_b64_tr_b16 v[162:163], %[wo] offset:512\n\t" \
    "ds_read_b64_tr_b16 v[164:165], v205 offset:0\n\t" \
    "ds_read_b64_tr_b16 v[166:167], v205 offset:512\n\t" \
    "ds_read_b64_tr_b16 v[168:169], v206 offset:0\n\t" \
    "ds_read_b64_tr_b16 v[170:171], v206 offset:512\n\t" \
    "ds_read_b64_tr_b16 v[172:173], v207 offset:0\n\t" \
    "ds_read_b64_tr_b16 v[174:175], v207 offset:512\n\t" \
    "ds_read_b128 v[128:131], %[xa] offset:0\n\t" \
    "ds_read_b128 v[132:135], %[xa] offset:2048\n\t" \
    "ds_read_b128 v[136:139], %[xa] offset:4096\n\t" \
    "ds_read_b128 v[140:143], %[xa] offset:6144\n\t" \
    "ds_read_b128 v[144:147], %[xa] offset:8192\n\t" \
    "ds_read_b128 v[148:151], %[xa] offset:10240\n\t" \
    "ds_read_b128 v[152:155], %[xa] offset:12288\n\t" \
    "ds_read_b128 v[156:159], %[xa] offset:14336\n\t" \
    "s_mov_b32 %[slot], 0\n\t" \
    "s_mov_b32 %[cnt], 15\n\t" \
    "s_mov_b32 m0, %[pm0]\n\t" \
    "s_waitcnt lgkmcnt(0)\n\t" \
    "1:\n\t" \
    "s_waitcnt lgkmcnt(5)\n\t" \
    "v_mfma_f32_16x16x32_bf16 v[0:3], v[160:163], v[128:131], v[0:3]\n\t" \
    "ds_read_b64_tr_b16 v[176:177], %[wo] offset:4096\n\t" \
    "v_mfma_f32_16x16x32_bf16 v[4:7], v[164:167], v[128:131], v[4:7]\n\t" \
    "ds_read_b64_tr_b16 v[178:179], %[wo] offset:4608\n\t" \
    "v_mfma_f32_16x16x32_bf16 v[8:11], v[168:171], v[128:131], v[8:11]\n\t" \
    "ds_read_b64_tr_b16 v[180:181], v205 offset:4096\n\t" \
    "v_mfma_f32_16x16x32_bf16 v[12:15], v[172:175], v[128:131], v[12:15]\n\t" \
    "ds_read_b128 v[128:131], v204 offset:0\n\t" \
    "v_mfma_f32_16x16x32_bf16 v[16:19], v[160:163], v[132:135], v[16:19]\n\t" \
    "ds_read_b64_tr_b16 v[182:183], v205 offset:4608\n\t" \
    "v_mfma_f32_16x16x32_bf16 v[20:23], v[164:167], v[132:135], v[20:23]\n\t" \
    "ds_read_b64_tr_b16 v[184:185], v206 offset:4096\n\t" \
    "v_mfma_f32_16x16x32_bf16 v[24:27], v[168:171], v[132:135], v[24:27]\n\t" \
    "ds_read_b64_tr_b16 v[186:187], v206 offset:4608\n\t" \
    "v_mfma_f32_16x16x32_bf16 v[28:31], v[172:175], v[132:135], v[28:31]\n\t" \
    "ds_read_b128 v[132:135], v204 offset:2048\n\t" \
    "s_waitcnt lgkmcnt(8)\n\t" \
    "v_mfma_f32_16x16x32_bf16 v[32:35], v[160:163], v[136:139], v[32:35]\n\t" \
    "ds_read_b64_tr_b16 v[188:189], v207 offset:4096\n\t" \
    "v_mfma_f32_16x16x32_bf16 v[36:39], v[164:167], v[136:139], v[36:39]\n\t" \
    "ds_read_b64_tr_b16 v[190:191], v207 offset:4608\n\t" \
    "v_mfma_f32_16x16x32_bf16 v[40:43], v[168:171], v[136:139], v[40:43]\n\t" \
    "ds_read_b128 v[192:195], %[exo]\n\t" \
    "v_mfma_f32_16x16x32_bf16 v[44:47], v[172:175], v[136:139], v[44:47]\n\t" \
    "ds_read_b128 v[136:139], v204 offset:4096\n\t" \
    "v_mfma_f32_16x16x32_bf16 v[48:51], v[160:163], v[140:143], v[48:51]\n\t" \
    "ds_read_b64_tr_b16 v[196:197], %[eo0]\n\t" \
    "v_mfma_f32_16x16x32_bf16 v[52:55], v[164:167], v[140:143], v[52:55]\n\t" \
    "ds_read_b64_tr_b16 v[198:199], %[eo1]\n\t" \
    "v_mfma_f32_16x16x32_bf16 v[56:59], v[168:171], v[140:143], v[56:59]\n\t" \
    "ds_read_b128 v[200:203], %[go]\n\t" \
    "v_mfma_f32_16x16x32_bf16 v[60:63], v[172:175], v[140:143], v[60:63]\n\t" \
    "ds_read_b128 v[140:143], v204 offset:6144\n\t" \
    "v_mfma_f32_16x16x32_bf16 v[64:67], v[160:163], v[144:147], v[64:67]\n\t" \
    "v_add_u32 %[go], 0x80, %[go]\n\t" \
    "v_mfma_f32_16x16x32_bf16 v[68:71], v[164:167], v[144:147], v[68:71]\n\t" \
    "v_mfma_f32_16x16x32_bf16 v[72:75], v[168:171], v[144:147], v[72:75]\n\t" \
    "v_mfma_f32_16x16x32_bf16 v[76:79], v[172:175], v[144:147], v[76:79]\n\t" \
    "ds_read_b128 v[144:147], v204 offset:8192\n\t" \
    "v_mfma_f32_16x16x32_bf16 v[80:83], v[160:163], v[148:151], v[80:83]\n\t" \
    "v_mfma_f32_16x16x32_bf16 v[84:87], v[164:167], v[148:151], v[84:87]\n\t" \
    "v_mfma_f32_16x16x32_bf16 v[88:91], v[168:171], v[148:151], v[88:91]\n\t" \
    "v_mfma_f32_16x16x32_bf16 v[92:95], v[172:175], v[148:151], v[92:95]\n\t" \
    "ds_read_b128 v[148:151], v204 offset:10240\n\t" \
    "v_mfma_f32_16x16x32_bf16 v[96:99], v[160:163], v[152:155], v[96:99]\n\t" \
    "s_waitcnt lgkmcnt(3)\n\t" \
    "v_mfma_f32_16x16x32_bf16 %[ea], v[196:199], v[192:195], %[ea]\n\t" \
    "v_mfma_f32_16x16x32_bf16 v[100:103], v[164:167], v[152:155], v[100:103]\n\t" \
    "v_mfma_f32_16x16x32_bf16 v[104:107], v[168:171], v[152:155], v[104:107]\n\t" \
    "v_mfma_f32_16x16x32_bf16 v[108:111], v[172:175], v[152:155], v[108:111]\n\t" \
    "ds_read_b128 v[152:155], v204 offset:12288\n\t" \
    "v_mfma_f32_16x16x32_bf16 v[112:115], v[160:163], v[156:159], v[112:115]\n\t" \
    "v_mfma_f32_16x16x32_bf16 %[ga], v[200:203], v[192:195], %[ga]\n\t" \
    "v_mfma_f32_16x16x32_bf16 v[116:119], v[164:167], v[156:159], v[116:119]\n\t" \
    "v_mfma_f32_16x16x32_bf16 v[120:123], v[168:171], v[156:159], v[120:123]\n\t" \
    "v_mfma_f32_16x16x32_bf16 v[124:127], v[172:175], v[156:159], v[124:127]\n\t" \
    "ds_read_b128 v[156:159], v204 offset:14336\n\t" \
    "v_mfma_f32_16x16x32_bf16 v[0:3], v[176:179], v[128:131], v[0:3]\n\t" \
    "s_add_u32 %[slot], %[slot], 1\n\t" \
    "s_cmp_eq_u32 %[slot], 3\n\t" \
    "s_mov_b32 %[dl], 51200\n\t" \
    "s_cselect_b32 %[dl], 0xfffe7000, %[dl]\n\t" \
    "s_cselect_b32 %[slot], 0, %[slot]\n\t" \
    "v_mfma_f32_16x16x32_bf16 v[4:7], v[180:183], v[128:131], v[4:7]\n\t" \
    "v_add_u32 %[xa], %[dl], %[xa]\n\t" \
    "v_add_u32 %[wo], %[dl], %[wo]\n\t" \
    "v_mfma_f32_16x16x32_bf16 v[8:11], v[184:187], v[128:131], v[8:11]\n\t" \
    "v_xor_b32 v205, 32, %[wo]\n\t" \
    "v_xor_b32 v206, 64, %[wo]\n\t" \
    "v_mfma_f32_16x16x32_bf16 v[12:15], v[188:191], v[128:131], v[12:15]\n\t" \
    "s_waitcnt lgkmcnt(0)\n\t" \
    "s_barrier\n\t" \
    "ds_read_b128 v[128:131], %[xa] offset:0\n\t" \
    "v_mfma_f32_16x16x32_bf16 v[16:19], v[176:179], v[132:135], v[16:19]\n\t" \
    "ds_read_b64_tr_b16 v[160:161], %[wo] offset:0\n\t" \
    "v_xor_b32 v207, 0x60, %[wo]\n\t" \
    "v_mfma_f32_16x16x32_bf16 v[20:23], v[180:183], v[132:135], v[20:23]\n\t" \
    "ds_read_b64_tr_b16 v[162:163], %[wo] offset:512\n\t" \
    "v_mfma_f32_16x16x32_bf16 v[24:27], v[184:187], v[132:135], v[24:27]\n\t" \
    "ds_read_b64_tr_b16 v[164:165], v205 offset:0\n\t" \
    "v_mfma_f32_16x16x32_bf16 v[28:31], v[188:191], v[132:135], v[28:31]\n\t" \
    "ds_read_b128 v[132:135], %[xa] offset:2048\n\t" \
    "v_xor_b32 v204, 64, %[xa]\n\t" \
    "v_mfma_f32_16x16x32_bf16 v[32:35], v[176:179], v[136:139], v[32:35]\n\t" \
    "ds_read_b64_tr_b16 v[166:167], v205 offset:512\n\t" \
    "v_mfma_f32_16x16x32_bf16 v[36:39], v[180:183], v[136:139], v[36:39]\n\t" \
    "ds_read_b64_tr_b16 v[168:169], v206 offset:0\n\t" \
    "v_mfma_f32_16x16x32_bf16 v[40:43], v[184:187], v[136:139], v[40:43]\n\t" \
    "ds_read_b64_tr_b16 v[170:171], v206 offset:512\n\t" \
    "v_mfma_f32_16x16x32_bf16 v[44:47], v[188:191], v[136:139], v[44:47]\n\t" \
    "ds_read_b128 v[136:139], %[xa] offset:4096\n\t" \
    "v_add_u32 %[exo], %[dl], %[exo]\n\t" \
    "v_mfma_f32_16x16x32_bf16 v[48:51], v[176:179], v[140:143], v[48:51]\n\t" \
    "ds_read_b64_tr_b16 v[172:173], v207 offset:0\n\t" \
    "v_mfma_f32_16x16x32_bf16 v[52:55], v[180:183], v[140:143], v[52:55]\n\t" \
    "ds_read_b64_tr_b16 v[174:175], v207 offset:512\n\t" \
    "v_mfma_f32_16x16x32_bf16 v[56:59], v[184:187], v[140:143], v[56:59]\n\t" \
    "v_mfma_f32_16x16x32_bf16 v[60:63], v[188:191], v[140:143], v[60:63]\n\t" \
    "ds_read_b128 v[140:143], %[xa] offset:6144\n\t" \
    "v_add_u32 %[eo0], %[dl], %[eo0]\n\t" \
    "v_mfma_f32_16x16x32_bf16 v[64:67], v[176:179], v[144:147], v[64:67]\n\t" \
    "v_mfma_f32_16x16x32_bf16 v[68:71], v[180:183], v[144:147], v[68:71]\n\t" \
    "v_mfma_f32_16x16x32_bf16 v[72:75], v[184:187], v[144:147], v[72:75]\n\t" \
    "v_mfma_f32_16x16x32_bf16 v[76:79], v[188:191], v[144:147], v[76:79]\n\t" \
    "ds_read_b128 v[144:147], %[xa] offset:8192\n\t" \
    "v_add_u32 %[eo1], %[dl], %[eo1]\n\t" \
    "v_mfma_f32_16x16x32_bf16 v[80:83], v[176:179], v[148:151], v[80:83]\n\t" \
    "v_mfma_f32_16x16x32_bf16 v[84:87], v[180:183], v[148:151], v[84:87]\n\t" \
    "s_cmp_lt_u32 %[cnt], 4\n\t" \
    "s_cbranch_scc1 6f\n\t" \
    "global_load_lds_dword v[210:211], off\n\t" \
    "6:\n\t" \
    "v_mfma_f32_16x16x32_bf16 v[88:91], v[184:187], v[148:151], v[88:91]\n\t" \
    "v_mfma_f32_16x16x32_bf16 v[92:95], v[188:191], v[148:151], v[92:95]\n\t" \
    "ds_read_b128 v[148:151], %[xa] offset:10240\n\t" \
    "v_add_co_u32 v210, vcc, %[pst], v210\n\t" \
    "v_mfma_f32_16x16x32_bf16 v[96:99], v[176:179], v[152:155], v[96:99]\n\t" \
    "v_mfma_f32_16x16x32_bf16 v[100:103], v[180:183], v[152:155], v[100:103]\n\t" \
    "v_mfma_f32_16x16x32_bf16 v[104:107], v[184:187], v[152:155], v[104:107]\n\t" \
    "v_mfma_f32_16x16x32_bf16 v[108:111], v[188:191], v[152:155], v[108:111]\n\t" \
    "ds_read_b128 v[152:155], %[xa] offset:12288\n\t" \
    "v_addc_co_u32 v211, vcc, 0, v211, vcc\n\t" \
    "v_mfma_f32_16x16x32_bf16 v[112:115], v[176:179], v[156:159], v[112:115]\n\t" \
    "v_mfma_f32_16x16x32_bf16 v[116:119], v[180:183], v[156:159], v[116:119]\n\t" \
    "v_mfma_f32_16x16x32_bf16 v[120:123], v[184:187], v[156:159], v[120:123]\n\t" \
    "v_mfma_f32_16x16x32_bf16 v[124:127], v[188:191], v[156:159], v[124:127]\n\t" \
    "ds_read_b128 v[156:159], %[xa] offset:14336\n\t" \
    "s_sub_u32 %[cnt], %[cnt], 1\n\t" \
    "s_cmp_lg_u32 %[cnt], 0\n\t" \
    "s_cbranch_scc1 1b\n\t" \
    "s_waitcnt lgkmcnt(5)\n\t" \
    "v_mfma_f32_16x16x32_bf16 v[0:3], v[160:163], v[128:131], v[0:3]\n\t" \
    "ds_read_b64_tr_b16 v[176:177], %[wo] offset:4096\n\t" \
    "v_mfma_f32_16x16x32_bf16 v[4:7], v[164:167], v[128:131], v[4:7]\n\t" \
    "ds_read_b64_tr_b16 v[178:179], %[wo] offset:4608\n\t" \
    "v_mfma_f32_16x16x32_bf16 v[8:11], v[168:171], v[128:131], v[8:11]\n\t" \
    "ds_read_b64_tr_b16 v[180:181], v205 offset:4096\n\t" \
    "v_mfma_f32_16x16x32_bf16 v[12:15], v[172:175], v[128:131], v[12:15]\n\t" \
    "ds_read_b128 v[128:131], v204 offset:0\n\t" \
    "v_mfma_f32_16x16x32_bf16 v[16:19], v[160:163], v[132:135], v[16:19]\n\t" \
    "ds_read_b64_tr_b16 v[182:183], v205 offset:4608\n\t" \
    "v_mfma_f32_16x16x32_bf16 v[20:23], v[164:167], v[132:135], v[20:23]\n\t" \
    "ds_read_b64_tr_b16 v[184:185], v206 offset:4096\n\t" \
    "v_mfma_f32_16x16x32_bf16 v[24:27], v[168:171], v[132:135], v[24:27]\n\t" \
    "ds_read_b64_tr_b16 v[186:187], v206 offset:4608\n\t" \
    "v_mfma_f32_16x16x32_bf16 v[28:31], v[172:175], v[132:135], v[28:31]\n\t" \
    "ds_read_b128 v[132:135], v204 offset:2048\n\t" \
    "s_waitcnt lgkmcnt(8)\n\t" \
    "v_mfma_f32_16x16x32_bf16 v[32:35], v[160:163], v[136:139], v[32:35]\n\t" \
    "ds_read_b64_tr_b16 v[188:189], v207 offset:4096\n\t" \
    "v_mfma_f32_16x16x32_bf16 v[36:39], v[164:167], v[136:139], v[36:39]\n\t" \
    "ds_read_b64_tr_b16 v[190:191], v207 offset:4608\n\t" \
    "v_mfma_f32_16x16x32_bf16 v[40:43], v[168:171], v[136:139], v[40:43]\n\t" \
    "ds_read_b128 v[192:195], %[exo]\n\t" \
    "v_mfma_f32_16x16x32_bf16 v[44:47], v[172:175], v[136:139], v[44:47]\n\t" \
    "ds_read_b128 v[136:139], v204 offset:4096\n\t" \
    "v_mfma_f32_16x16x32_bf16 v[48:51], v[160:163], v[140:143], v[48:51]\n\t" \
    "ds_read_b64_tr_b16 v[196:197], %[eo0]\n\t" \
    "v_mfma_f32_16x16x32_bf16 v[52:55], v[164:167], v[140:143], v[52:55]\n\t" \
    "ds_read_b64_tr_b16 v[198:199], %[eo1]\n\t" \
    "v_mfma_f32_16x16x32_bf16 v[56:59], v[168:171], v[140:143], v[56:59]\n\t" \
    "ds_read_b128 v[200:203], %[go]\n\t" \
    "v_mfma_f32_16x16x32_bf16 v[60:63], v[172:175], v[140:143], v[60:63]\n\t" \
    "ds_read_b128 v[140:143], v204 offset:6144\n\t" \
    "v_mfma_f32_16x16x32_bf16 v[64:67], v[160:163], v[144:147], v[64:67]\n\t" \
    "v_add_u32 %[go], 0x80, %[go]\n\t" \
    "v_mfma_f32_16x16x32_bf16 v[68:71], v[164:167], v[144:147], v[68:71]\n\t" \
    "v_mfma_f32_16x16x32_bf16 v[72:75], v[168:171], v[144:147], v[72:75]\n\t" \
    "v_mfma_f32_16x16x32_bf16 v[76:79], v[172:175], v[144:147], v[76:79]\n\t" \
    "ds_read_b128 v[144:147], v204 offset:8192\n\t" \
    "v_mfma_f32_16x16x32_bf16 v[80:83], v[160:163], v[148:151], v[80:83]\n\t" \
    "v_mfma_f32_16x16x32_bf16 v[84:87], v[164:167], v[148:151], v[84:87]\n\t" \
    "v_mfma_f32_16x16x32_bf16 v[88:91], v[168:171], v[148:151], v[88:91]\n\t" \
    "v_mfma_f32_16x16x32_bf16 v[92:95], v[172:175], v[148:151], v[92:95]\n\t" \
    "ds_read_b128 v[148:151], v204 offset:10240\n\t" \
    "v_mfma_f32_16x16x32_bf16 v[96:99], v[160:163], v[152:155], v[96:99]\n\t" \
    "s_waitcnt lgkmcnt(3)\n\t" \
    "v_mfma_f32_16x16x32_bf16 %[ea], v[196:199], v[192:195], %[ea]\n\t" \
    "v_mfma_f32_16x16x32_bf16 v[100:103], v[164:167], v[152:155], v[100:103]\n\t" \
    "v_mfma_f32_16x16x32_bf16 v[104:107], v[168:171], v[152:155], v[104:107]\n\t" \
    "v_mfma_f32_16x16x32_bf16 v[108:111], v[172:175], v[152:155], v[108:111]\n\t" \
    "ds_read_b128 v[152:155], v204 offset:12288\n\t" \
    "v_mfma_f32_16x16x32_bf16 v[112:115], v[160:163], v[156:159], v[112:115]\n\t" \
    "v_mfma_f32_16x16x32_bf16 %[ga], v[200:203], v[192:195], %[ga]\n\t" \
    "v_mfma_f32_16x16x32_bf16 v[116:119], v[164:167], v[156:159], v[116:119]\n\t" \
    "v_mfma_f32_16x16x32_bf16 v[120:123], v[168:171], v[156:159], v[120:123]\n\t" \
    "v_mfma_f32_16x16x32_bf16 v[124:127], v[172:175], v[156:159], v[124:127]\n\t" \
    "ds_read_b128 v[156:159], v204 offset:14336\n\t" \
    "v_mfma_f32_16x16x32_bf16 v[0:3], v[176:179], v[128:131], v[0:3]\n\t" \
    "v_mfma_f32_16x16x32_bf16 v[4:7], v[180:183], v[128:131], v[4:7]\n\t" \
    "v_mfma_f32_16x16x32_bf16 v[8:11], v[184:187], v[128:131], v[8:11]\n\t" \
    "v_mfma_f32_16x16x32_bf16 v[12:15], v[188:191], v[128:131], v[12:15]\n\t" \
    "v_mfma_f32_16x16x32_bf16 v[16:19], v[176:179], v[132:135], v[16:19]\n\t" \
    "v_mfma_f32_16x16x32_bf16 v[20:23], v[180:183], v[132:135], v[20:23]\n\t" \
    "v_mfma_f32_16x16x32_bf16 v[24:27], v[184:187], v[132:135], v[24:27]\n\t" \
    "v_mfma_f32_16x16x32_bf16 v[28:31], v[188:191], v[132:135], v[28:31]\n\t" \
    "s_waitcnt lgkmcnt(0)\n\t" \
    "v_mfma_f32_16x16x32_bf16 v[32:35], v[176:179], v[136:139], v[32:35]\n\t" \
    "v_mfma_f32_16x16x32_bf16 v[36:39], v[180:183], v[136:139], v[36:39]\n\t" \
    "v_mfma_f32_16x16x32_bf16 v[40:43], v[184:187], v[136:139], v[40:43]\n\t" \
    "v_mfma_f32_16x16x32_bf16 v[44:47], v[188:191], v[136:139], v[44:47]\n\t" \
    "v_mfma_f32_16x16x32_bf16 v[48:51], v[176:179], v[140:143], v[48:51]\n\t" \
    "v_mfma_f32_16x16x32_bf16 v[52:55], v[180:183], v[140:143], v[52:55]\n\t" \
    "v_mfma_f32_16x16x32_bf16 v[56:59], v[184:187], v[140:143], v[56:59]\n\t" \
    "v_mfma_f32_16x16x32_bf16 v[60:63], v[188:191], v[140:143], v[60:63]\n\t" \
    "v_mfma_f32_16x16x32_bf16 v[64:67], v[176:179], v[144:147], v[64:67]\n\t" \
    "v_mfma_f32_16x16x32_bf16 v[68:71], v[180:183], v[144:147], v[68:71]\n\t" \
    "v_mfma_f32_16x16x32_bf16 v[72:75], v[184:187], v[144:147], v[72:75]\n\t" \
    "v_mfma_f32_16x16x32_bf16 v[76:79], v[188:191], v[144:147], v[76:79]\n\t" \
    "v_mfma_f32_16x16x32_bf16 v[80:83], v[176:179], v[148:151], v[80:83]\n\t" \
    "v_mfma_f32_16x16x32_bf16 v[84:87], v[180:183], v[148:151], v[84:87]\n\t" \
    "v_mfma_f32_16x16x32_bf16 v[88:91], v[184:187], v[148:151], v[88:91]\n\t" \
    "v_mfma_f32_16x16x32_bf16 v[92:95], v[188:191], v[148:151], v[92:95]\n\t" \
    "v_mfma_f32_16x16x32_bf16 v[96:99], v[176:179], v[152:155], v[96:99]\n\t" \
    "v_mfma_f32_16x16x32_bf16 v[100:103], v[180:183], v[152:155], v[100:103]\n\t" \
    "v_mfma_f32_16x16x32_bf16 v[104:107], v[184:187], v[152:155], v[104:107]\n\t" \
    "v_mfma_f32_16x16x32_bf16 v[108:111], v[188:191], v[152:155], v[108:111]\n\t" \
    "v_mfma_f32_16x16x32_bf16 v[112:115], v[176:179], v[156:159], v[112:115]\n\t" \
    "v_mfma_f32_16x16x32_bf16 v[116:119], v[180:183], v[156:159], v[116:119]\n\t" \
    "v_mfma_f32_16x16x32_bf16 v[120:123], v[184:187], v[156:159], v[120:123]\n\t" \
    "v_mfma_f32_16x16x32_bf16 v[124:127], v[188:191], v[156:159], v[124:127]\n\t" \
    "s_nop 15\n\t"
#define PROJ_ASM_V \
    "s_nop 1\n\t" \
    "v_xor_b32 v204, 64, %[xa]\n\t" \
    "v_xor_b32 v205, 32, %[wo]\n\t" \
    "v_xor_b32 v206, 64, %[wo]\n\t" \
    "v_xor_b32 v207, 0x60, %[wo]\n\t" \
    "ds_read_b64_tr_b16 v[160:161], %[wo] offset:0\n\t" \
    "ds_read_b64_tr_b16 v[162:163], %[wo] offset:512\n\t" \
    "ds_read_b64_tr_b16 v[164:165], v205 offset:0\n\t" \
    "ds_read_b64_tr_b16 v[166:167], v205 offset:512\n\t" \
    "ds_read_b64_tr_b16 v[168:169], v206 offset:0\n\t" \
    "ds_read_b64_tr_b16 v[170:171], v206 offset:512\n\t" \
    "ds_read_b64_tr_b16 v[172:173], v207 offset:0\n\t" \
    "ds_read_b64_tr_b16 v[174:175], v207 offset:512\n\t" \
    "ds_read_b128 v[128:131], %[xa] offset:0\n\t" \
    "ds_read_b128 v[132:135], %[xa] offset:2048\n\t" \
    "ds_read_b128 v[136:139], %[xa] offset:4096\n\t" \
    "ds_read_b128 v[140:143], %[xa] offset:6144\n\t" \
    "ds_read_b128 v[144:147], %[xa] offset:8192\n\t" \
    "ds_read_b128 v[148:151], %[xa] offset:10240\n\t" \
    "ds_read_b128 v[152:155], %[xa] offset:12288\n\t" \
    "ds_read_b128 v[156:159], %[xa] offset:14336\n\t" \
    "s_mov_b32 %[slot], 0\n\t" \
    "s_mov_b32 %[cnt], 15\n\t" \
    "s_mov_b32 m0, %[pm0]\n\t" \
    "s_waitcnt lgkmcnt(0)\n\t" \
    "1:\n\t" \
    "s_waitcnt lgkmcnt(5)\n\t" \
    "v_mfma_f32_16x16x32_bf16 v[0:3], v[128:131], v[160:163], v[0:3]\n\t" \
    "ds_read_b64_tr_b16 v[176:177], %[wo] offset:4096\n\t" \
    "v_mfma_f32_16x16x32_bf16 v[4:7], v[128:131], v[164:167], v[4:7]\n\t" \
    "ds_read_b64_tr_b16 v[178:179], %[wo] offset:4608\n\t" \
    "v_mfma_f32_16x16x32_bf16 v[8:11], v[128:131], v[168:171], v[8:11]\n\t" \
    "ds_read_b64_tr_b16 v[180:181], v205 offset:4096\n\t" \
    "v_mfma_f32_16x16x32_bf16 v[12:15], v[128:131], v[172:175], v[12:15]\n\t" \
    "ds_read_b128 v[128:131], v204 offset:0\n\t" \
    "v_mfma_f32_16x16x32_bf16 v[16:19], v[132:135], v[160:163], v[16:19]\n\t" \
    "ds_read_b64_tr_b16 v[182:183], v205 offset:4608\n\t" \
    "v_mfma_f32_16x16x32_bf16 v[20:23], v[132:135], v[164:167], v[20:23]\n\t" \
    "ds_read_b64_tr_b16 v[184:185], v206 offset:4096\n\t" \
    "v_mfma_f32_16x16x32_bf16 v[24:27], v[132:135], v[168:171], v[24:27]\n\t" \
    "ds_read_b64_tr_b16 v[186:187], v206 offset:4608\n\t" \
    "v_mfma_f32_16x16x32_bf16 v[28:31], v[132:135], v[172:175], v[28:31]\n\t" \
    "ds_read_b128 v[132:135], v204 offset:2048\n\t" \
    "s_waitcnt lgkmcnt(8)\n\t" \
    "v_mfma_f32_16x16x32_bf16 v[32:35], v[136:139], v[160:163], v[32:35]\n\t" \
    "ds_read_b64_tr_b16 v[188:189], v207 offset:4096\n\t" \
    "v_mfma_f32_16x16x32_bf16 v[36:39], v[136:139], v[164:167], v[36:39]\n\t" \
    "ds_read_b64_tr_b16 v[190:191], v207 offset:4608\n\t" \
    "v_mfma_f32_16x16x32_bf16 v[40:43], v[136:139], v[168:171], v[40:43]\n\t" \
    "ds_read_b128 v[192:195], %[exo]\n\t" \
    "v_mfma_f32_16x16x32_bf16 v[44:47], v[136:139], v[172:175], v[44:47]\n\t" \
    "ds_read_b128 v[136:139], v204 offset:4096\n\t" \
    "v_mfma_f32_16x16x32_bf16 v[48:51], v[140:143], v[160:163], v[48:51]\n\t" \
    "ds_read_b64_tr_b16 v[196:197], %[eo0]\n\t" \
    "v_mfma_f32_16x16x32_bf16 v[52:55], v[140:143], v[164:167], v[52:55]\n\t" \
    "ds_read_b64_tr_b16 v[198:199], %[eo1]\n\t" \
    "v_mfma_f32_16x16x32_bf16 v[56:59], v[140:143], v[168:171], v[56:59]\n\t" \
    "ds_read_b128 v[200:203], %[go]\n\t" \
    "v_mfma_f32_16x16x32_bf16 v[60:63], v[140:143], v[172:175], v[60:63]\n\t" \
    "ds_read_b128 v[140:143], v204 offset:6144\n\t" \
    "v_mfma_f32_16x16x32_bf16 v[64:67], v[144:147], v[160:163], v[64:67]\n\t" \
    "v_add_u32 %[go], 0x80, %[go]\n\t" \
    "v_mfma_f32_16x16x32_bf16 v[68:71], v[144:147], v[164:167], v[68:71]\n\t" \
    "v_mfma_f32_16x16x32_bf16 v[72:75], v[144:147], v[168:171], v[72:75]\n\t" \
    "v_mfma_f32_16x16x32_bf16 v[76:79], v[144:147], v[172:175], v[76:79]\n\t" \
    "ds_read_b128 v[144:147], v204 offset:8192\n\t" \
    "v_mfma_f32_16x16x32_bf16 v[80:83], v[148:151], v[160:163], v[80:83]\n\t" \
    "v_mfma_f32_16x16x32_bf16 v[84:87], v[148:151], v[164:167], v[84:87]\n\t" \
    "v_mfma_f32_16x16x32_bf16 v[88:91], v[148:151], v[168:171], v[88:91]\n\t" \
    "v_mfma_f32_16x16x32_bf16 v[92:95], v[148:151], v[172:175], v[92:95]\n\t" \
    "ds_read_b128 v[148:151], v204 offset:10240\n\t" \
    "v_mfma_f32_16x16x32_bf16 v[96:99], v[152:155], v[160:163], v[96:99]\n\t" \
    "s_waitcnt lgkmcnt(3)\n\t" \
    "v_mfma_f32_16x16x32_bf16 %[ea], v[196:199], v[192:195], %[ea]\n\t" \
    "v_mfma_f32_16x16x32_bf16 v[100:103], v[152:155], v[164:167], v[100:103]\n\t" \
    "v_mfma_f32_16x16x32_bf16 v[104:107], v[152:155], v[168:171], v[104:107]\n\t" \
    "v_mfma_f32_16x16x32_bf16 v[108:111], v[152:155], v[172:175], v[108:111]\n\t" \
    "ds_read_b128 v[152:155], v204 offset:12288\n\t" \
    "v_mfma_f32_16x16x32_bf16 v[112:115], v[156:159], v[160:163], v[112:115]\n\t" \
    "v_mfma_f32_16x16x32_bf16 %[ga], v[200:203], v[192:195], %[ga]\n\t" \
    "v_mfma_f32_16x16x32_bf16 v[116:119], v[156:159], v[164:167], v[116:119]\n\t" \
    "v_mfma_f32_16x16x32_bf16 v[120:123], v[156:159], v[168:171], v[120:123]\n\t" \
    "v_mfma_f32_16x16x32_bf16 v[124:127], v[156:159], v[172:175], v[124:127]\n\t" \
    "ds_read_b128 v[156:159], v204 offset:14336\n\t" \
    "v_mfma_f32_16x16x32_bf16 v[0:3], v[128:131], v[176:179], v[0:3]\n\t" \
    "s_add_u32 %[slot], %[slot], 1\n\t" \
    "s_cmp_eq_u32 %[slot], 3\n\t" \
    "s_mov_b32 %[dl], 51200\n\t" \
    "s_cselect_b32 %[dl], 0xfffe7000, %[dl]\n\t" \
    "s_cselect_b32 %[slot], 0, %[slot]\n\t" \
    "v_mfma_f32_16x16x32_bf16 v[4:7], v[128:131], v[180:183], v[4:7]\n\t" \
    "v_add_u32 %[xa], %[dl], %[xa]\n\t" \
    "v_add_u32 %[wo], %[dl], %[wo]\n\t" \
    "v_mfma_f32_16x16x32_bf16 v[8:11], v[128:131], v[184:187], v[8:11]\n\t" \
    "v_xor_b32 v205, 32, %[wo]\n\t" \
    "v_xor_b32 v206, 64, %[wo]\n\t" \
    "v_mfma_f32_16x16x32_bf16 v[12:15], v[128:131], v[188:191], v[12:15]\n\t" \
    "s_waitcnt lgkmcnt(0)\n\t" \
    "s_barrier\n\t" \
    "ds_read_b128 v[128:131], %[xa] offset:0\n\t" \
    "v_mfma_f32_16x16x32_bf16 v[16:19], v[132:135], v[176:179], v[16:19]\n\t" \
    "ds_read_b64_tr_b16 v[160:161], %[wo] offset:0\n\t" \
    "v_xor_b32 v207, 0x60, %[wo]\n\t" \
    "v_mfma_f32_16x16x32_bf16 v[20:23], v[132:135], v[180:183], v[20:23]\n\t" \
    "ds_read_b64_tr_b16 v[162:163], %[wo] offset:512\n\t" \
    "v_mfma_f32_16x16x32_bf16 v[24:27], v[132:135], v[184:187], v[24:27]\n\t" \
    "ds_read_b64_tr_b16 v[164:165], v205 offset:0\n\t" \
    "v_mfma_f32_16x16x32_bf16 v[28:31], v[132:135], v[188:191], v[28:31]\n\t" \
    "ds_read_b128 v[132:135], %[xa] offset:2048\n\t" \
    "v_xor_b32 v204, 64, %[xa]\n\t" \
    "v_mfma_f32_16x16x32_bf16 v[32:35], v[136:139], v[176:179], v[32:35]\n\t" \
    "ds_read_b64_tr_b16 v[166:167], v205 offset:512\n\t" \
    "v_mfma_f32_16x16x32_bf16 v[36:39], v[136:139], v[180:183], v[36:39]\n\t" \
    "ds_read_b64_tr_b16 v[168:169], v206 offset:0\n\t" \
    "v_mfma_f32_16x16x32_bf16 v[40:43], v[136:139], v[184:187], v[40:43]\n\t" \
    "ds_read_b64_tr_b16 v[170:171], v206 offset:512\n\t" \
    "v_mfma_f32_16x16x32_bf16 v[44:47], v[136:139], v[188:191], v[44:47]\n\t" \
    "ds_read_b128 v[136:139], %[xa] offset:4096\n\t" \
    "v_add_u32 %[exo], %[dl], %[exo]\n\t" \
    "v_mfma_f32_16x16x32_bf16 v[48:51], v[140:143], v[176:179], v[48:51]\n\t" \
    "ds_read_b64_tr_b16 v[172:173], v207 offset:0\n\t" \
    "v_mfma_f32_16x16x32_bf16 v[52:55], v[140:143], v[180:183], v[52:55]\n\t" \
    "ds_read_b64_tr_b16 v[174:175], v207 offset:512\n\t" \
    "v_mfma_f32_16x16x32_bf16 v[56:59], v[140:143], v[184:187], v[56:59]\n\t" \
    "v_mfma_f32_16x16x32_bf16 v[60:63], v[140:143], v[188:191], v[60:63]\n\t" \
    "ds_read_b128 v[140:143], %[xa] offset:6144\n\t" \
    "v_add_u32 %[eo0], %[dl], %[eo0]\n\t" \
    "v_mfma_f32_16x16x32_bf16 v[64:67], v[144:147], v[176:179], v[64:67]\n\t" \
    "v_mfma_f32_16x16x32_bf16 v[68:71], v[144:147], v[180:183], v[68:71]\n\t" \
    "v_mfma_f32_16x16x32_bf16 v[72:75], v[144:147], v[184:187], v[72:75]\n\t" \
    "v_mfma_f32_16x16x32_bf16 v[76:79], v[144:147], v[188:191], v[76:79]\n\t" \
    "ds_read_b128 v[144:147], %[xa] offset:8192\n\t" \
    "v_add_u32 %[eo1], %[dl], %[eo1]\n\t" \
    "v_mfma_f32_16x16x32_bf16 v[80:83], v[148:151], v[176:179], v[80:83]\n\t" \
    "v_mfma_f32_16x16x32_bf16 v[84:87], v[148:151], v[180:183], v[84:87]\n\t" \
    "s_cmp_lt_u32 %[cnt], 4\n\t" \
    "s_cbranch_scc1 6f\n\t" \
    "global_load_lds_dword v[210:211], off\n\t" \
    "6:\n\t" \
    "v_mfma_f32_16x16x32_bf16 v[88:91], v[148:151], v[184:187], v[88:91]\n\t" \
    "v_mfma_f32_16x16x32_bf16 v[92:95], v[148:151], v[188:191], v[92:95]\n\t" \
    "ds_read_b128 v[148:151], %[xa] offset:10240\n\t" \
    "v_add_co_u32 v210, vcc, %[pst], v210\n\t" \
    "v_mfma_f32_16x16x32_bf16 v[96:99], v[152:155], v[176:179], v[96:99]\n\t" \
    "v_mfma_f32_16x16x32_bf16 v[100:103], v[152:155], v[180:183], v[100:103]\n\t" \
    "v_mfma_f32_16x16x32_bf16 v[104:107], v[152:155], v[184:187], v[104:107]\n\t" \
    "v_mfma_f32_16x16x32_bf16 v[108:111], v[152:155], v[188:191], v[108:111]\n\t" \
    "ds_read_b128 v[152:155], %[xa] offset:12288\n\t" \
    "v_addc_co_u32 v211, vcc, 0, v211, vcc\n\t" \
    "v_mfma_f32_16x16x32_bf16 v[112:115], v[156:159], v[176:179], v[112:115]\n\t" \
    "v_mfma_f32_16x16x32_bf16 v[116:119], v[156:159], v[180:183], v[116:119]\n\t" \
    "v_mfma_f32_16x16x32_bf16 v[120:123], v[156:159], v[184:187], v[120:123]\n\t" \
    "v_mfma_f32_16x16x32_bf16 v[124:127], v[156:159], v[188:191], v[124:127]\n\t" \
    "ds_read_b128 v[156:159], %[xa] offset:14336\n\t" \
    "s_sub_u32 %[cnt], %[cnt], 1\n\t" \
    "s_cmp_lg_u32 %[cnt], 0\n\t" \
    "s_cbranch_scc1 1b\n\t" \
    "s_waitcnt lgkmcnt(5)\n\t" \
    "v_mfma_f32_16x16x32_bf16 v[0:3], v[128:131], v[160:163], v[0:3]\n\t" \
    "ds_read_b64_tr_b16 v[176:177], %[wo] offset:4096\n\t" \
    "v_mfma_f32_16x16x32_bf16 v[4:7], v[128:131], v[164:167], v[4:7]\n\t" \
    "ds_read_b64_tr_b16 v[178:179], %[wo] offset:4608\n\t" \
    "v_mfma_f32_16x16x32_bf16 v[8:11], v[128:131], v[168:171], v[8:11]\n\t" \
    "ds_read_b64_tr_b16 v[180:181], v205 offset:4096\n\t" \
    "v_mfma_f32_16x16x32_bf16 v[12:15], v[128:131], v[172:175], v[12:15]\n\t" \
    "ds_read_b128 v[128:131], v204 offset:0\n\t" \
    "v_mfma_f32_16x16x32_bf16 v[16:19], v[132:135], v[160:163], v[16:19]\n\t" \
    "ds_read_b64_tr_b16 v[182:183], v205 offset:4608\n\t" \
    "v_mfma_f32_16x16x32_bf16 v[20:23], v[132:135], v[164:167], v[20:23]\n\t" \
    "ds_read_b64_tr_b16 v[184:185], v206 offset:4096\n\t" \
    "v_mfma_f32_16x16x32_bf16 v[24:27], v[132:135], v[168:171], v[24:27]\n\t" \
    "ds_read_b64_tr_b16 v[186:187], v206 offset:4608\n\t" \
    "v_mfma_f32_16x16x32_bf16 v[28:31], v[132:135], v[172:175], v[28:31]\n\t" \
    "ds_read_b128 v[132:135], v204 offset:2048\n\t" \
    "s_waitcnt lgkmcnt(8)\n\t" \
    "v_mfma_f32_16x16x32_bf16 v[32:35], v[136:139], v[160:163], v[32:35]\n\t" \
    "ds_read_b64_tr_b16 v[188:189], v207 offset:4096\n\t" \
    "v_mfma_f32_16x16x32_bf16 v[36:39], v[136:139], v[164:167], v[36:39]\n\t" \
    "ds_read_b64_tr_b16 v[190:191], v207 offset:4608\n\t" \
    "v_mfma_f32_16x16x32_bf16 v[40:43], v[136:139], v[168:171], v[40:43]\n\t" \
    "ds_read_b128 v[192:195], %[exo]\n\t" \
    "v_mfma_f32_16x16x32_bf16 v[44:47], v[136:139], v[172:175], v[44:47]\n\t" \
    "ds_read_b128 v[136:139], v204 offset:4096\n\t" \
    "v_mfma_f32_16x16x32_bf16 v[48:51], v[140:143], v[160:163], v[48:51]\n\t" \
    "ds_read_b64_tr_b16 v[196:197], %[eo0]\n\t" \
    "v_mfma_f32_16x16x32_bf16 v[52:55], v[140:143], v[164:167], v[52:55]\n\t" \
    "ds_read_b64_tr_b16 v[198:199], %[eo1]\n\t" \
    "v_mfma_f32_16x16x32_bf16 v[56:59], v[140:143], v[168:171], v[56:59]\n\t" \
    "ds_read_b128 v[200:203], %[go]\n\t" \
    "v_mfma_f32_16x16x32_bf16 v[60:63], v[140:143], v[172:175], v[60:63]\n\t" \
    "ds_read_b128 v[140:143], v204 offset:6144\n\t" \
    "v_mfma_f32_16x16x32_bf16 v[64:67], v[144:147], v[160:163], v[64:67]\n\t" \
    "v_add_u32 %[go], 0x80, %[go]\n\t" \
    "v_mfma_f32_16x16x32_bf16 v[68:71], v[144:147], v[164:167], v[68:71]\n\t" \
    "v_mfma_f32_16x16x32_bf16 v[72:75], v[144:147], v[168:171], v[72:75]\n\t" \
    "v_mfma_f32_16x16x32_bf16 v[76:79], v[144:147], v[172:175], v[76:79]\n\t" \
    "ds_read_b128 v[144:147], v204 offset:8192\n\t" \
    "v_mfma_f32_16x16x32_bf16 v[80:83], v[148:151], v[160:163], v[80:83]\n\t" \
    "v_mfma_f32_16x16x32_bf16 v[84:87], v[148:151], v[164:167], v[84:87]\n\t" \
    "v_mfma_f32_16x16x32_bf16 v[88:91], v[148:151], v[168:171], v[88:91]\n\t" \
    "v_mfma_f32_16x16x32_bf16 v[92:95], v[148:151], v[172:175], v[92:95]\n\t" \
    "ds_read_b128 v[148:151], v204 offset:10240\n\t" \
    "v_mfma_f32_16x16x32_bf16 v[96:99], v[152:155], v[160:163], v[96:99]\n\t" \
    "s_waitcnt lgkmcnt(3)\n\t" \
    "v_mfma_f32_16x16x32_bf16 %[ea], v[196:199], v[192:195], %[ea]\n\t" \
    "v_mfma_f32_16x16x32_bf16 v[100:103], v[152:155], v[164:167], v[100:103]\n\t" \
    "v_mfma_f32_16x16x32_bf16 v[104:107], v[152:155], v[168:171], v[104:107]\n\t" \
    "v_mfma_f32_16x16x32_bf16 v[108:111], v[152:155], v[172:175], v[108:111]\n\t" \
    "ds_read_b128 v[152:155], v204 offset:12288\n\t" \
    "v_mfma_f32_16x16x32_bf16 v[112:115], v[156:159], v[160:163], v[112:115]\n\t" \
    "v_mfma_f32_16x16x32_bf16 %[ga], v[200:203], v[192:195], %[ga]\n\t" \
    "v_mfma_f32_16x16x32_bf16 v[116:119], v[156:159], v[164:167], v[116:119]\n\t" \
    "v_mfma_f32_16x16x32_bf16 v[120:123], v[156:159], v[168:171], v[120:123]\n\t" \
    "v_mfma_f32_16x16x32_bf16 v[124:127], v[156:159], v[172:175], v[124:127]\n\t" \
    "ds_read_b128 v[156:159], v204 offset:14336\n\t" \
    "v_mfma_f32_16x16x32_bf16 v[0:3], v[128:131], v[176:179], v[0:3]\n\t" \
    "v_mfma_f32_16x16x32_bf16 v[4:7], v[128:131], v[180:183], v[4:7]\n\t" \
    "v_mfma_f32_16x16x32_bf16 v[8:11], v[128:131], v[184:187], v[8:11]\n\t" \
    "v_mfma_f32_16x16x32_bf16 v[12:15], v[128:131], v[188:191], v[12:15]\n\t" \
    "v_mfma_f32_16x16x32_bf16 v[16:19], v[132:135], v[176:179], v[16:19]\n\t" \
    "v_mfma_f32_16x16x32_bf16 v[20:23], v[132:135], v[180:183], v[20:23]\n\t" \
    "v_mfma_f32_16x16x32_bf16 v[24:27], v[132:135], v[184:187], v[24:27]\n\t" \
    "v_mfma_f32_16x16x32_bf16 v[28:31], v[132:135], v[188:191], v[28:31]\n\t" \
    "s_waitcnt lgkmcnt(0)\n\t" \
    "v_mfma_f32_16x16x32_bf16 v[32:35], v[136:139], v[176:179], v[32:35]\n\t" \
    "v_mfma_f32_16x16x32_bf16 v[36:39], v[136:139], v[180:183], v[36:39]\n\t" \
    "v_mfma_f32_16x16x32_bf16 v[40:43], v[136:139], v[184:187], v[40:43]\n\t" \
    "v_mfma_f32_16x16x32_bf16 v[44:47], v[136:139], v[188:191], v[44:47]\n\t" \
    "v_mfma_f32_16x16x32_bf16 v[48:51], v[140:143], v[176:179], v[48:51]\n\t" \
    "v_mfma_f32_16x16x32_bf16 v[52:55], v[140:143], v[180:183], v[52:55]\n\t" \
    "v_mfma_f32_16x16x32_bf16 v[56:59], v[140:143], v[184:187], v[56:59]\n\t" \
    "v_mfma_f32_16x16x32_bf16 v[60:63], v[140:143], v[188:191], v[60:63]\n\t" \
    "v_mfma_f32_16x16x32_bf16 v[64:67], v[144:147], v[176:179], v[64:67]\n\t" \
    "v_mfma_f32_16x16x32_bf16 v[68:71], v[144:147], v[180:183], v[68:71]\n\t" \
    "v_mfma_f32_16x16x32_bf16 v[72:75], v[144:147], v[184:187], v[72:75]\n\t" \
    "v_mfma_f32_16x16x32_bf16 v[76:79], v[144:147], v[188:191], v[76:79]\n\t" \
    "v_mfma_f32_16x16x32_bf16 v[80:83], v[148:151], v[176:179], v[80:83]\n\t" \
    "v_mfma_f32_16x16x32_bf16 v[84:87], v[148:151], v[180:183], v[84:87]\n\t" \
    "v_mfma_f32_16x16x32_bf16 v[88:91], v[148:151], v[184:187], v[88:91]\n\t" \
    "v_mfma_f32_16x16x32_bf16 v[92:95], v[148:151], v[188:191], v[92:95]\n\t" \
    "v_mfma_f32_16x16x32_bf16 v[96:99], v[152:155], v[176:179], v[96:99]\n\t" \
    "v_mfma_f32_16x16x32_bf16 v[100:103], v[152:155], v[180:183], v[100:103]\n\t" \
    "v_mfma_f32_16x16x32_bf16 v[104:107], v[152:155], v[184:187], v[104:107]\n\t" \
    "v_mfma_f32_16x16x32_bf16 v[108:111], v[152:155], v[188:191], v[108:111]\n\t" \
    "v_mfma_f32_16x16x32_bf16 v[112:115], v[156:159], v[176:179], v[112:115]\n\t" \
    "v_mfma_f32_16x16x32_bf16 v[116:119], v[156:159], v[180:183], v[116:119]\n\t" \
    "v_mfma_f32_16x16x32_bf16 v[120:123], v[156:159], v[184:187], v[120:123]\n\t" \
    "v_mfma_f32_16x16x32_bf16 v[124:127], v[156:159], v[188:191], v[124:127]\n\t" \
    "s_nop 15\n\t"
#define PROJ_CLOBBERS "v128", "v129", "v130", "v131", "v132", "v133", "v134", "v135", "v136", "v137", "v138", "v139", "v140", "v141", "v142", "v143", "v144", "v145", "v146", "v147", "v148", "v149", "v150", "v151", "v152", "v153", "v154", "v155", "v156", "v157", "v158", "v159", "v160", "v161", "v162", "v163", "v164", "v165", "v166", "v167", "v168", "v169", "v170", "v171", "v172", "v173", "v174", "v175", "v176", "v177", "v178", "v179", "v180", "v181", "v182", "v183", "v184", "v185", "v186", "v187", "v188", "v189", "v190", "v191", "v192", "v193", "v194", "v195", "v196", "v197", "v198", "v199", "v200", "v201", "v202", "v203", "v204", "v205", "v206", "v207", "scc", "vcc", "m0", "memory"
#define OUT_ASM_HEAD \
    "s_nop 1\n\t" \
    "v_xor_b32 v80, 32, %[wa]\n\t" \
    "ds_read_b64_tr_b16 v[64:65], %[wa] offset:0\n\t" \
    "ds_read_b64_tr_b16 v[66:67], %[wa] offset:1024\n\t" \
    "ds_read_b128 v[32:35], %[oa] offset:0\n\t" \
    "ds_read_b64_tr_b16 v[68:69], v80 offset:0\n\t" \
    "ds_read_b64_tr_b16 v[70:71], v80 offset:1024\n\t" \
    "ds_read_b128 v[36:39], %[oa] offset:256\n\t" \
    "ds_read_b128 v[40:43], %[oa] offset:4096\n\t" \
    "ds_read_b128 v[44:47], %[oa] offset:4352\n\t" \
    "s_waitcnt lgkmcnt(0)\n\t" \
    "1:\n\t" \
    "s_waitcnt lgkmcnt(3)\n\t" \
    "v_mfma_f32_16x16x32_bf16 v[0:3], v[64:67], v[32:35], v[0:3]\n\t" \
    "ds_read_b64_tr_b16 v[72:73], %[wa] offset:4096\n\t" \
    "v_mfma_f32_16x16x32_bf16 v[4:7], v[68:71], v[32:35], v[4:7]\n\t" \
    "ds_read_b64_tr_b16 v[74:75], %[wa] offset:5120\n\t" \
    "s_waitcnt lgkmcnt(4)\n\t" \
    "v_mfma_f32_16x16x32_bf16 v[8:11], v[64:67], v[36:39], v[8:11]\n\t" \
    "ds_read_b128 v[48:51], %[oa] offset:2048\n\t" \
    "v_mfma_f32_16x16x32_bf16 v[12:15], v[68:71], v[36:39], v[12:15]\n\t" \
    "ds_read_b64_tr_b16 v[76:77], v80 offset:4096\n\t" \
    "s_waitcnt lgkmcnt(5)\n\t" \
    "v_mfma_f32_16x16x32_bf16 v[16:19], v[64:67], v[40:43], v[16:19]\n\t" \
    "ds_read_b64_tr_b16 v[78:79], v80 offset:5120\n\t" \
    "v_mfma_f32_16x16x32_bf16 v[20:23], v[68:71], v[40:43], v[20:23]\n\t" \
    "ds_read_b128 v[52:55], %[oa] offset:2304\n\t" \
    "s_waitcnt lgkmcnt(6)\n\t" \
    "v_mfma_f32_16x16x32_bf16 v[24:27], v[64:67], v[44:47], v[24:27]\n\t" \
    "ds_read_b128 v[56:59], %[oa] offset:6144\n\t" \
    "v_mfma_f32_16x16x32_bf16 v[28:31], v[68:71], v[44:47], v[28:31]\n\t" \
    "ds_read_b128 v[60:63], %[oa] offset:6400\n\t" \
    "s_waitcnt lgkmcnt(3)\n\t" \
    "v_mfma_f32_16x16x32_bf16 v[0:3], v[72:75], v[48:51], v[0:3]\n\t" \
    "s_add_u32 %[slot], %[slot], 1\n\t" \
    "s_cmp_eq_u32 %[slot], 6\n\t" \
    "s_mov_b32 %[dl], 24576\n\t" \
    "s_cselect_b32 %[dl], 0xfffe2000, %[dl]\n\t" \
    "s_cselect_b32 %[slot], 0, %[slot]\n\t" \
    "v_add_u32 %[oa], %[dl], %[oa]\n\t" \
    "v_add_u32 %[wa], %[dl], %[wa]\n\t" \
    "s_waitcnt lgkmcnt(0)\n\t" \
    "s_barrier\n\t" \
    "v_xor_b32 v80, 32, %[wa]\n\t" \
    "ds_read_b64_tr_b16 v[64:65], %[wa] offset:0\n\t" \
    "v_mfma_f32_16x16x32_bf16 v[4:7], v[76:79], v[48:51], v[4:7]\n\t" \
    "ds_read_b64_tr_b16 v[66:67], %[wa] offset:1024\n\t" \
    "v_mfma_f32_16x16x32_bf16 v[8:11], v[72:75], v[52:55], v[8:11]\n\t" \
    "ds_read_b128 v[32:35], %[oa] offset:0\n\t" \
    "v_mfma_f32_16x16x32_bf16 v[12:15], v[76:79], v[52:55], v[12:15]\n\t" \
    "ds_read_b64_tr_b16 v[68:69], v80 offset:0\n\t" \
    "v_mfma_f32_16x16x32_bf16 v[16:19], v[72:75], v[56:59], v[16:19]\n\t" \
    "ds_read_b64_tr_b16 v[70:71], v80 offset:1024\n\t" \
    "v_mfma_f32_16x16x32_bf16 v[20:23], v[76:79], v[56:59], v[20:23]\n\t" \
    "ds_read_b128 v[36:39], %[oa] offset:256\n\t" \
    "v_mfma_f32_16x16x32_bf16 v[24:27], v[72:75], v[60:63], v[24:27]\n\t" \
    "ds_read_b128 v[40:43], %[oa] offset:4096\n\t" \
    "v_mfma_f32_16x16x32_bf16 v[28:31], v[76:79], v[60:63], v[28:31]\n\t" \
    "ds_read_b128 v[44:47], %[oa] offset:4352\n\t" \
    "s_cmp_lg_u32 %[cnt], 9\n\t" \
    "s_cbranch_scc1 6f\n\t" \
    "s_waitcnt vmcnt(0)\n\t" \
    "buffer_store_dword %[vtag], %[vfo], %[srd], 0 offen sc1\n\t" \
    "6:\n\t" \
    "s_sub_u32 %[cnt], %[cnt], 1\n\t" \
    "s_cmp_lg_u32 %[cnt], 0\n\t" \
    "s_cbranch_scc1 1b\n\t" \
    "s_waitcnt lgkmcnt(0)\n\t" \
    "s_nop 15\n\t"
#define OUT_ASM_TAIL \
    "s_nop 1\n\t" \
    "v_xor_b32 v80, 32, %[wa]\n\t" \
    "ds_read_b64_tr_b16 v[64:65], %[wa] offset:0\n\t" \
    "ds_read_b64_tr_b16 v[66:67], %[wa] offset:1024\n\t" \
    "ds_read_b128 v[32:35], %[oa] offset:0\n\t" \
    "ds_read_b64_tr_b16 v[68:69], v80 offset:0\n\t" \
    "ds_read_b64_tr_b16 v[70:71], v80 offset:1024\n\t" \
    "ds_read_b128 v[36:39], %[oa] offset:256\n\t" \
    "ds_read_b128 v[40:43], %[oa] offset:4096\n\t" \
    "ds_read_b128 v[44:47], %[oa] offset:4352\n\t" \
    "s_waitcnt lgkmcnt(0)\n\t" \
    "1:\n\t" \
    "s_waitcnt lgkmcnt(3)\n\t" \
    "v_mfma_f32_16x16x32_bf16 v[0:3], v[64:67], v[32:35], v[0:3]\n\t" \
    "ds_read_b64_tr_b16 v[72:73], %[wa] offset:4096\n\t" \
    "v_mfma_f32_16x16x32_bf16 v[4:7], v[68:71], v[32:35], v[4:7]\n\t" \
    "ds_read_b64_tr_b16 v[74:75], %[wa] offset:5120\n\t" \
    "s_waitcnt lgkmcnt(4)\n\t" \
    "v_mfma_f32_16x16x32_bf16 v[8:11], v[64:67], v[36:39], v[8:11]\n\t" \
    "ds_read_b128 v[48:51], %[oa] offset:2048\n\t" \
    "v_mfma_f32_16x16x32_bf16 v[12:15], v[68:71], v[36:39], v[12:15]\n\t" \
    "ds_read_b64_tr_b16 v[76:77], v80 offset:4096\n\t" \
    "s_waitcnt lgkmcnt(5)\n\t" \
    "v_mfma_f32_16x16x32_bf16 v[16:19], v[64:67], v[40:43], v[16:19]\n\t" \
    "ds_read_b64_tr_b16 v[78:79], v80 offset:5120\n\t" \
    "v_mfma_f32_16x16x32_bf16 v[20:23], v[68:71], v[40:43], v[20:23]\n\t" \
    "ds_read_b128 v[52:55], %[oa] offset:2304\n\t" \
    "s_waitcnt lgkmcnt(6)\n\t" \
    "v_mfma_f32_16x16x32_bf16 v[24:27], v[64:67], v[44:47], v[24:27]\n\t" \
    "ds_read_b128 v[56:59], %[oa] offset:6144\n\t" \
    "v_mfma_f32_16x16x32_bf16 v[28:31], v[68:71], v[44:47], v[28:31]\n\t" \
    "ds_read_b128 v[60:63], %[oa] offset:6400\n\t" \
    "s_waitcnt lgkmcnt(3)\n\t" \
    "v_mfma_f32_16x16x32_bf16 v[0:3], v[72:75], v[48:51], v[0:3]\n\t" \
    "s_add_u32 %[slot], %[slot], 1\n\t" \
    "s_cmp_eq_u32 %[slot], 6\n\t" \
    "s_mov_b32 %[dl], 24576\n\t" \
    "s_cselect_b32 %[dl], 0xfffe2000, %[dl]\n\t" \
    "s_cselect_b32 %[slot], 0, %[slot]\n\t" \
    "v_add_u32 %[oa], %[dl], %[oa]\n\t" \
    "v_add_u32 %[wa], %[dl], %[wa]\n\t" \
    "s_waitcnt lgkmcnt(0)\n\t" \
    "s_barrier\n\t" \
    "v_xor_b32 v80, 32, %[wa]\n\t" \
    "ds_read_b64_tr_b16 v[64:65], %[wa] offset:0\n\t" \
    "v_mfma_f32_16x16x32_bf16 v[4:7], v[76:79], v[48:51], v[4:7]\n\t" \
    "ds_read_b64_tr_b16 v[66:67], %[wa] offset:1024\n\t" \
    "v_mfma_f32_16x16x32_bf16 v[8:11], v[72:75], v[52:55], v[8:11]\n\t" \
    "ds_read_b128 v[32:35], %[oa] offset:0\n\t" \
    "v_mfma_f32_16x16x32_bf16 v[12:15], v[76:79], v[52:55], v[12:15]\n\t" \
    "ds_read_b64_tr_b16 v[68:69], v80 offset:0\n\t" \
    "v_mfma_f32_16x16x32_bf16 v[16:19], v[72:75], v[56:59], v[16:19]\n\t" \
    "ds_read_b64_tr_b16 v[70:71], v80 offset:1024\n\t" \
    "v_mfma_f32_16x16x32_bf16 v[20:23], v[76:79], v[56:59], v[20:23]\n\t" \
    "ds_read_b128 v[36:39], %[oa] offset:256\n\t" \
    "v_mfma_f32_16x16x32_bf16 v[24:27], v[72:75], v[60:63], v[24:27]\n\t" \
    "ds_read_b128 v[40:43], %[oa] offset:4096\n\t" \
    "v_mfma_f32_16x16x32_bf16 v[28:31], v[76:79], v[60:63], v[28:31]\n\t" \
    "ds_read_b128 v[44:47], %[oa] offset:4352\n\t" \
    "s_sub_u32 %[cnt], %[cnt], 1\n\t" \
    "s_cmp_lg_u32 %[cnt], 0\n\t" \
    "s_cbranch_scc1 1b\n\t" \
    "s_waitcnt lgkmcnt(3)\n\t" \
    "v_mfma_f32_16x16x32_bf16 v[0:3], v[64:67], v[32:35], v[0:3]\n\t" \
    "ds_read_b64_tr_b16 v[72:73], %[wa] offset:4096\n\t" \
    "v_mfma_f32_16x16x32_bf16 v[4:7], v[68:71], v[32:35], v[4:7]\n\t" \
    "ds_read_b64_tr_b16 v[74:75], %[wa] offset:5120\n\t" \
    "s_waitcnt lgkmcnt(4)\n\t" \
    "v_mfma_f32_16x16x32_bf16 v[8:11], v[64:67], v[36:39], v[8:11]\n\t" \
    "ds_read_b128 v[48:51], %[oa] offset:2048\n\t" \
    "v_mfma_f32_16x16x32_bf16 v[12:15], v[68:71], v[36:39], v[12:15]\n\t" \
    "ds_read_b64_tr_b16 v[76:77], v80 offset:4096\n\t" \
    "s_waitcnt lgkmcnt(5)\n\t" \
    "v_mfma_f32_16x16x32_bf16 v[16:19], v[64:67], v[40:43], v[16:19]\n\t" \
    "ds_read_b64_tr_b16 v[78:79], v80 offset:5120\n\t" \
    "v_mfma_f32_16x16x32_bf16 v[20:23], v[68:71], v[40:43], v[20:23]\n\t" \
    "ds_read_b128 v[52:55], %[oa] offset:2304\n\t" \
    "s_waitcnt lgkmcnt(6)\n\t" \
    "v_mfma_f32_16x16x32_bf16 v[24:27], v[64:67], v[44:47], v[24:27]\n\t" \
    "ds_read_b128 v[56:59], %[oa] offset:6144\n\t" \
    "v_mfma_f32_16x16x32_bf16 v[28:31], v[68:71], v[44:47], v[28:31]\n\t" \
    "ds_read_b128 v[60:63], %[oa] offset:6400\n\t" \
    "s_waitcnt lgkmcnt(3)\n\t" \
    "v_mfma_f32_16x16x32_bf16 v[0:3], v[72:75], v[48:51], v[0:3]\n\t" \
    "v_mfma_f32_16x16x32_bf16 v[4:7], v[76:79], v[48:51], v[4:7]\n\t" \
    "s_waitcnt lgkmcnt(2)\n\t" \
    "v_mfma_f32_16x16x32_bf16 v[8:11], v[72:75], v[52:55], v[8:11]\n\t" \
    "v_mfma_f32_16x16x32_bf16 v[12:15], v[76:79], v[52:55], v[12:15]\n\t" \
    "s_waitcnt lgkmcnt(1)\n\t" \
    "v_mfma_f32_16x16x32_bf16 v[16:19], v[72:75], v[56:59], v[16:19]\n\t" \
    "v_mfma_f32_16x16x32_bf16 v[20:23], v[76:79], v[56:59], v[20:23]\n\t" \
    "s_waitcnt lgkmcnt(0)\n\t" \
    "v_mfma_f32_16x16x32_bf16 v[24:27], v[72:75], v[60:63], v[24:27]\n\t" \
    "v_mfma_f32_16x16x32_bf16 v[28:31], v[76:79], v[60:63], v[28:31]\n\t" \
    "s_waitcnt lgkmcnt(0)\n\t" \
    "s_nop 15\n\t"
#define OUT_CLOBBERS "v32", "v33", "v34", "v35", "v36", "v37", "v38", "v39", "v40", "v41", "v42", "v43", "v44", "v45", "v46", "v47", "v48", "v49", "v50", "v51", "v52", "v53", "v54", "v55", "v56", "v57", "v58", "v59", "v60", "v61", "v62", "v63", "v64", "v65", "v66", "v67", "v68", "v69", "v70", "v71", "v72", "v73", "v74", "v75", "v76", "v77", "v78", "v79", "v80", "scc", "memory"
constexpr int P_STAGE = 16384 + 32768 + 2048;
constexpr int P_GATE = 3 * P_STAGE;
constexpr int E_RED = P_GATE + 2048, E_XFL = E_RED + 5120, P_DUMMY = E_XFL + 64;
constexpr int E_QA = 0, E_QB = 16384;
constexpr int B_OFF = 65536, B_HEAD = 49152;
constexpr int A_SLOT = 32768;
constexpr int A_XCH = 73728;
constexpr int O_STAGE = 16384 + 8192;
constexpr int LDS_BYTES = 163840;

DI unsigned read_tag(const char* ws) {
    unsigned e; asm volatile("s_load_dword %0, %1, %2\n\ts_waitcnt lgkmcnt(0)" : "=&s"(e) : "s"(ws), "n"(WS_EPOCH) : "memory"); return e + 1u; }
constexpr int CODE_LINES = 280;
DI unsigned long long step_line(const void* x, const void* wq, const void* wqkv, int j, unsigned s, unsigned* stride) {
    const unsigned xcd = blockIdx.x & 7, li = blockIdx.x >> 3;
    if (j >= 32) { *stride = 128u; return (unsigned long long)(uintptr_t)x + (blockIdx.x * 8u + (unsigned)(j - 32)) * 2048u + s * 128u; }
    const unsigned n = li * 32u + (unsigned)j, p = n >> 8, r = (n >> 2) & 63u, col = ((xcd >> 1) * 4u + (n & 3u)) * 128u;
    if (p == 0) { *stride = 64u * 2048u; return (unsigned long long)(uintptr_t)wq + (s * 64u + r) * 2048u + col; }
    *stride = 64u * 6144u; return (unsigned long long)(uintptr_t)wqkv + (s * 64u + r) * 6144u + (p - 1u) * 2048u + col;
}

template <int KIND>
DI void proj_pack(const f32x16 (&acc)[8], u32x4 (&pk)[2][4][2], srd_t wsr, unsigned dst) {
    constexpr bool VT = KIND == 3; constexpr float sc = KIND == 1 ? QK_SCALE : 1.0f;
#pragma unroll
    for (int j = 0; j < 2; ++j)
#pragma unroll
        for (int i = 0; i < 4; ++i)
#pragma unroll
            for (int sx = 0; sx < 2; ++sx) {
                const f32x16& A = VT ? acc[2 * i + sx] : acc[2 * i]; const f32x16& B = VT ? acc[2 * i + sx] : acc[2 * i + 1];
                const int ja = VT ? 2 * j : 2 * j + sx, jb = VT ? 2 * j + 1 : 2 * j + sx;
                auto r0 = __builtin_amdgcn_permlane32_swap(pack2(A[4 * ja] * sc, A[4 * ja + 1] * sc), pack2(B[4 * jb] * sc, B[4 * jb + 1] * sc), false, false);
                auto r1 = __builtin_amdgcn_permlane32_swap(pack2(A[4 * ja + 2] * sc, A[4 * ja + 3] * sc), pack2(B[4 * jb + 2] * sc, B[4 * jb + 3] * sc), false, false);
                pk[j][i][sx] = (u32x4){r0[0], r1[0], r0[1], r1[1]};
                if (KIND >= 2) st16(wsr, dst + (unsigned)(j * 64 + i) * 4096u + (unsigned)sx * 1024u, pk[j][i][sx]);
            }
}

template <bool VT>
DI void proj_consume(lds_cptr L, srd_t wsr, int sg, int lane, int hp, int tq, unsigned tag, char* ws, const void* x, const void* wq, const void* wqkv) {
    const int ett = hp >> 2; const bool extk = (hp & 3) < 2; f32x4 ea = f32x4{}, ga4 = f32x4{};
    const int c = lane & 15, g = lane >> 4, q = c >> 2, pp = lane & 3;
    f32x16 acc[8];
#pragma unroll
    for (int i = 0; i < 8; ++i) acc[i] = f32x16{};
    unsigned xa = (unsigned)(c * 128 + ((g ^ (c >> 1)) << 4));
    unsigned wo = (unsigned)(16384 + sg * 8192 + (8 * g + q) * 128 + (((((q >> 1) + 2 * (g & 1)) << 1) | (pp >> 1)) << 4) + 8 * (pp & 1));
    const int th = sg & 1, kh = sg >> 1, erow = ett * 32 + th * 16 + c;
    unsigned exo = (unsigned)(erow * 128 + (((kh * 4 + g) ^ ((erow >> 1) & 7)) << 4));
    const int ek = kh * 32 + 8 * g + q;
    unsigned eo0 = (unsigned)(49152 + (ek ^ ((g & 1) << 2)) * 32 + 8 * pp), eo1 = (unsigned)(49152 + ((ek + 4) ^ ((g & 1) << 2)) * 32 + 8 * pp);
    unsigned go = (unsigned)(P_GATE + (kh * 32 + 8 * g) * 2);
    {
        const unsigned l0 = (unsigned)(uintptr_t)L;
        xa += l0; wo += l0; exo += l0; eo0 += l0; eo1 += l0; go += l0;
    }
    if (kh == 0 && lane == 0) *(__attribute__((address_space(3))) volatile unsigned*)(L + E_XFL + th * 4) = 0u;
#pragma unroll
    for (int i = 0; i < 8; ++i) PIN(acc[i]);
    PIN(ea); PIN(ga4); PIN(xa); PIN(wo); PIN(exo); PIN(eo0); PIN(eo1); PIN(go);
    WAITL_BAR();
    {
        unsigned slot, cnt, dl, pst;
        unsigned long long pa = step_line(x, wq, wqkv, lane < 8 ? sg * 8 + lane : 32 + sg * 2 + (lane > 8), 4u, &pst);
        const unsigned pm0 = (unsigned)(uintptr_t)L + P_DUMMY;
#define PROJ_OPERANDS \
        : "+{v[0:15]}"(acc[0]), "+{v[16:31]}"(acc[1]), "+{v[32:47]}"(acc[2]), "+{v[48:63]}"(acc[3]), \
          "+{v[64:79]}"(acc[4]), "+{v[80:95]}"(acc[5]), "+{v[96:111]}"(acc[6]), "+{v[112:127]}"(acc[7]), [ea] "+&v"(ea), [ga] "+&v"(ga4), \
          [xa] "+&v"(xa), [wo] "+&v"(wo), [exo] "+&v"(exo), [eo0] "+&v"(eo0), [eo1] "+&v"(eo1), [go] "+&v"(go), \
          [slot] "=&s"(slot), [cnt] "=&s"(cnt), [dl] "=&s"(dl), "+{v[210:211]}"(pa) \
        : [pst] "v"(pst), [pm0] "s"(pm0) : PROJ_CLOBBERS
        if (VT) asm volatile(PROJ_ASM_V PROJ_OPERANDS); else asm volatile(PROJ_ASM_QK PROJ_OPERANDS);
#undef PROJ_OPERANDS
    }
    WAITV(0);
    {
        typedef __attribute__((address_space(3))) volatile unsigned* lds_vu;
        typedef __attribute__((address_space(3))) float* lds_f;
        const lds_vu flag = (lds_vu)(L + E_XFL + th * 4);
        const lds_cptr red = L + E_RED + th * 2560 + kh * 1280;
        lds_st4(red + (c * 16 + 4 * g) * 4, __builtin_bit_cast(u32x4, ea));
        *(lds_f)(red + 1024 + lane * 4) = ga4[0];
        asm volatile("s_waitcnt lgkmcnt(0)" ::: "memory");
        if (kh == 1) {
            if (lane == 0) *flag = 1u;
        } else {
            for (int it = 0; it < 1000000 && rfl(*flag) == 0u; ++it) __builtin_amdgcn_s_sleep(0);
            asm volatile("" ::: "memory");
            const int cg = hp & 3;
            const unsigned etile = (unsigned)(tq * 4 + ett);
            f32x4 e; unsigned off;
            if (extk) {
                e = (__builtin_bit_cast(f32x4, lds_u4(red + (c * 16 + 4 * g) * 4)) + __builtin_bit_cast(f32x4, lds_u4(red + 1280 + (c * 16 + 4 * g) * 4))) * QK_SCALE;
                off = WS_KVA + etile * 4096u + (unsigned)(cg & 1) * 1024u + (unsigned)(th * 16 + c + 32 * (g & 1)) * 16u + (unsigned)(g >> 1) * 8u;
            } else {
#pragma unroll
                for (int k = 0; k < 4; ++k) e[k] = *(lds_f)(red + ((4 * g + k) * 16 + c) * 4) + *(lds_f)(red + 1280 + ((4 * g + k) * 16 + c) * 4);
                off = WS_KVA + etile * 4096u + 2048u + (unsigned)th * 1024u + (unsigned)((cg & 1) * 16 + c + 32 * (g & 1)) * 16u + (unsigned)(g >> 1) * 8u;
            }
            __builtin_amdgcn_raw_buffer_store_b64((u32x2){pack2(e[0], e[1]), pack2(e[2], e[3])}, wsr, off, 0, SC1);
            if (cg == 0) {
                const float ga = ga4[0] + *(lds_f)(red + 1280 + 1024 + lane * 4);
                const float gt = 1.0f / (1.0f + __expf(-ga));
                if (g == 0) __builtin_amdgcn_raw_buffer_store_b32(__float_as_uint(gt), wsr, WS_G + (etile * 32 + th * 16 + c) * 4, 0, SC1);
            }
        }
    }
    const unsigned fl = (unsigned)(c + 16 * (lane >> 5) + 32 * (g & 1)) * 16u;
    const unsigned kvg = WS_KVB + (unsigned)(2 * hp * 64 + tq * 4) * 4096u + fl;
    u32x4 pk[2][4][2];
    if (VT) proj_pack<3>(acc, pk, wsr, kvg + 2048u);
    else if (sg == 2) proj_pack<2>(acc, pk, wsr, kvg);
    else if (sg == 1) proj_pack<1>(acc, pk, wsr, 0u);
    else proj_pack<0>(acc, pk, wsr, 0u);
    WAITVL_BAR();
    unsigned nfo = 0, nf = 0;
    if (sg == 0) {
        if (lane < 8) __builtin_amdgcn_raw_buffer_store_b32(tag, wsr, WS_FLAG1 + lane * 1024 + blockIdx.x * 4, 0, SC1);
        const int tn = tq - 1 - (lane & 1), nb_ = tn < 0 ? (int)blockIdx.x : (((hp & 3) | ((tn & 7) << 2)) * 8 + (((hp >> 2) << 1) | (tn >> 3)));
        nfo = WS_FLAG1 + (blockIdx.x & 7) * 1024u + (unsigned)nb_ * 4u;
        nf = __builtin_amdgcn_raw_buffer_load_b32(wsr, nfo, 0, SC1);
    }
#pragma unroll
    for (int j = 0; j < 2; ++j)
#pragma unroll
        for (int i = 0; i < 4; ++i) {
            const unsigned dst = (sg < 2 ? (unsigned)(sg * 16384 + (j * 4 + i) * 2048) : (unsigned)(B_OFF + j * B_HEAD + (8 + i) * 4096 + (VT ? 2048 : 0))) + fl;
            lds_st4(L + dst, pk[j][i][0]); lds_st4(L + dst + 1024, pk[j][i][1]);
        }
    if (sg == 0 && !__all(nf == tag)) poll_word(wsr, nfo, tag);
    WAITL_BAR();
}

__global__ __launch_bounds__(512, 2) void k_fused(const __hip_bfloat16* __restrict__ x, const __hip_bfloat16* __restrict__ wq,
                                                  const __hip_bfloat16* __restrict__ wk, const __hip_bfloat16* __restrict__ wv,
                                                  const __hip_bfloat16* __restrict__ wqkv, const __hip_bfloat16* __restrict__ gate_w,
                                                  const __hip_bfloat16* __restrict__ woA, const __hip_bfloat16* __restrict__ woB,
                                                  char* __restrict__ ws, __hip_bfloat16* __restrict__ out) {
    __shared__ __attribute__((aligned(1024))) char smem[LDS_BYTES];
    const lds_cptr L = (lds_cptr)smem;
    const unsigned lds0 = (unsigned)(uintptr_t)(lds_ptr)smem;
    const int lane = threadIdx.x & 63, w = rfl(threadIdx.x >> 6);
    if (w * 64 < CODE_LINES) {
        unsigned long long pc; const unsigned vo = (unsigned)min(w * 64 + lane, CODE_LINES - 1) * 128u, dd = lds0 + P_DUMMY + 1024;
        asm volatile("s_getpc_b64 %0\n\ts_mov_b32 m0, %2\n\ts_nop 0\n\tglobal_load_lds_dword %1, %0" : "=&s"(pc) : "v"(vo), "s"(dd) : "m0", "memory");
    }
    __builtin_amdgcn_sched_barrier(0);
    const int xcd = blockIdx.x & 7, li = blockIdx.x >> 3;
    const int hp = (xcd >> 1) * 4 + (li & 3), tq = (xcd & 1) * 8 + (li >> 2);
    const int cg = hp & 3;
    const srd_t wsr = make_srd(ws, WS_END);
    const unsigned lv = (unsigned)lane * 16u;
    unsigned tag = 0;
    if (w < 4) {
        if (w < 2) {
            unsigned st; const unsigned long long a = step_line(x, wq, wqkv, min(lane, 39), 2u + (unsigned)w, &st);
            asm volatile("s_mov_b32 m0, %1\n\ts_nop 0\n\tglobal_load_lds_dword %0, off" :: "v"(a), "s"(lds0 + P_DUMMY) : "m0", "memory");
        }
        tag = read_tag(ws);
        if (w == 3) proj_consume<true>(L, wsr, w, lane, hp, tq, tag, ws, x, wq, wqkv); else proj_consume<false>(L, wsr, w, lane, hp, tq, tag, ws, x, wq, wqkv);
    } else {
        const int p = w - 4;
        const unsigned ncols = p ? 3072u : 1024u;
        const srd_t xs = make_srd(x, SEQ * DM * 2);
        const srd_t wsd = make_srd(p ? wqkv : wq, DM * ncols * 2);
        const srd_t es = make_srd((cg & 2) ? wv : wk, DM * 32 * 2);
        unsigned xv[4], wv0, wv1;
#pragma unroll
        for (int i = 0; i < 4; ++i) { const int row = p * 32 + i * 8 + (lane >> 3); xv[i] = (unsigned)((tq * 128 + row) * 2048 + (((lane & 7) ^ ((row >> 1) & 7)) << 4)); }
        wv0 = (unsigned)(lane >> 3) * ncols * 2u + (unsigned)(((p ? p - 1 : 0) * 1024 + hp * 64) * 2 + (((lane & 7) ^ (((lane >> 4) & 1) << 1)) << 4));
        wv1 = wv0 ^ 64u;
        const int epos = (p & 1) * 32 + (lane >> 1), erow = epos ^ (((epos >> 3) & 1) << 2);
        const unsigned ev = (unsigned)(erow * 64 + (cg & 1) * 32 + (lane & 1) * 16);
        const unsigned wstep = 64u * ncols * 2u;
#define P_ISSUE(t, sl) do { const unsigned d_ = lds0 + (sl); \
        _Pragma("unroll") for (int i_ = 0; i_ < 4; ++i_) dma16(xs, xv[i_], (unsigned)(t) * 128u, d_ + p * 4096 + i_ * 1024); \
        _Pragma("unroll") for (int i_ = 0; i_ < 8; ++i_) dma16(wsd, (i_ & 1) ? wv1 : wv0, (unsigned)(t) * wstep + (unsigned)i_ * (wstep >> 3), d_ + 16384 + p * 8192 + i_ * 1024); \
        if (p < 2) dma16(es, ev, (unsigned)(t) * 4096u, d_ + 49152 + p * 1024); } while (0)
#define P_WAIT() do { if (p < 2) { WAITV(13); } else { WAITV(12); } } while (0)
        if (p < 2) dma16(make_srd(gate_w, DM * 2), (unsigned)(p * 1024) + lv, 0u, lds0 + P_GATE + p * 1024);
        P_ISSUE(0, 0);
        P_ISSUE(1, P_STAGE);
        tag = read_tag(ws);
        P_WAIT();
        BAR();
        unsigned s2 = 2 * P_STAGE;
        for (int t = 0; t < 15; ++t) {
            if (t < 14) { P_ISSUE(t + 2, s2); s2 = (s2 == 2 * P_STAGE) ? 0u : s2 + P_STAGE; P_WAIT(); } else { WAITV(0); }
            BAR();
        }
#undef P_ISSUE
#undef P_WAIT
        BAR();
        BAR();
    }
    const int grp = w >> 2, qi = 2 * (w & 3) + grp, head = 2 * hp + (qi >> 2), tile = tq * 4 + (qi & 3);
#define D4(so) "buffer_load_dwordx4 %[lv], %[srd], " so " offen sc1 lds\n\tbuffer_load_dwordx4 %[lv], %[srd], " so " offen offset:1024 sc1 lds\n\t" \
               "buffer_load_dwordx4 %[lv], %[srd], " so " offen offset:2048 sc1 lds\n\tbuffer_load_dwordx4 %[lv], %[srd], " so " offen offset:3072 sc1 lds\n\t"
    bf16x8 qx0, qx1, qy0, qy1, qb0, qb1; f32x16 bs0, bneg, bcf;
    const unsigned kvb = lds0 + B_OFF + (unsigned)((qi >> 2) * B_HEAD + (qi & 3) * 4096) + lv;
    {
        const int bl = 2 * (w & 3), bt = tq * 4 - 8 + bl;
        const unsigned bbase = WS_KVB + (unsigned)((2 * hp + (w >> 2)) * 64) * 4096u;
        const unsigned b0 = bbase + (unsigned)(bt < 0 ? tq * 4 : bt) * 4096u, b1 = bbase + (unsigned)(bt + 1 < 0 ? tq * 4 : bt + 1) * 4096u;
        const unsigned mb0 = lds0 + B_OFF + (unsigned)((w >> 2) * B_HEAD + bl * 4096), mb1 = mb0 + 4096;
        const unsigned vfl = w == 0 ? WS_FLAG1 + (unsigned)xcd * 1024u + lv : 0xFFFFFFF0u;
        asm volatile("s_mov_b32 m0, %[mb0]\n\ts_nop 0\n\t" D4("%[b0]") "s_mov_b32 m0, %[mb1]\n\ts_nop 0\n\t" D4("%[b1]")
                     :: [lv] "v"(lv), [srd] "s"(wsr), [mb0] "s"(mb0), [mb1] "s"(mb1), [b0] "s"(b0), [b1] "s"(b1) : "m0", "memory");
        qx0 = lds_frag(L + E_QA + qi * 2048 + lv); qx1 = lds_frag(L + E_QA + qi * 2048 + 1024 + lv);
        qy0 = lds_frag(L + E_QA + (qi ^ 1) * 2048 + lv); qy1 = lds_frag(L + E_QA + (qi ^ 1) * 2048 + 1024 + lv);
        qb0 = lds_frag(L + E_QB + qi * 2048 + lv); qb1 = lds_frag(L + E_QB + qi * 2048 + 1024 + lv);
        PIN(qx0); PIN(qx1); PIN(qy0); PIN(qy1); PIN(qb0); PIN(qb1);
        asm volatile(ATTN_BP_ASM
                     : "={v[128:143]}"(bs0), "={v[160:175]}"(bneg), "={v[176:191]}"(bcf)
                     : [q0] "v"(qb0), [q1] "v"(qb1), [kv] "v"(kvb)
                     : ATTN_B_CLOBBERS);
        u32x4 fl;
        asm volatile("buffer_load_dwordx4 %0, %1, %2, 0 offen sc1\n\ts_waitcnt vmcnt(0)" : "=&v"(fl) : "v"(vfl), "s"(wsr) : "memory");
        if (w == 0) {
            if (!__all(fl[0] == tag && fl[1] == tag && fl[2] == tag && fl[3] == tag)) poll_flags(wsr, vfl, tag);
            if (blockIdx.x == 0 && lane == 0) __builtin_amdgcn_raw_buffer_store_b32(tag, wsr, WS_EPOCH, 0, SC1);
        }
    }
    const unsigned one2 = (((lane & 3) == 0) && (((lane >> 4) & 1) == ((lane >> 2) & 1))) ? 0x3F803F80u : 0u;
    bf16x8 ones = as_frag((u32x4){one2, one2, one2, one2});
    const unsigned obase = WS_O + (unsigned)tile * 131072u + (unsigned)head * 2048u + lv;
    const unsigned asoff = WS_KVA + (unsigned)(grp * 32 + (w & 3)) * 4096u;
    f32x16 O = f32x16{}; f32x4 Ls = f32x4{};
    PIN(O); PIN(Ls); PIN(ones);
    WAITL_BAR();
    float gate;
    {
        const unsigned goff = WS_G + (unsigned)(tile * 32 + (lane & 31)) * 4u;
        const unsigned ma0 = lds0 + (unsigned)w * 4096u, ma1 = ma0 + A_SLOT, a1 = asoff + 16384u;
        asm volatile("buffer_load_dword %[gate], %[go], %[srd], 0 offen sc1\n\t"
                     "s_mov_b32 m0, %[ma0]\n\ts_nop 0\n\t" D4("%[a0]") "s_mov_b32 m0, %[ma1]\n\ts_nop 0\n\t" D4("%[a1]")
                     : [gate] "=&v"(gate)
                     : [lv] "v"(lv), [go] "v"(goff), [srd] "s"(wsr), [ma0] "s"(ma0), [ma1] "s"(ma1), [a0] "s"(asoff), [a1] "s"(a1)
                     : "m0", "memory");
    }
#undef D4
    {
        const unsigned nt = tile < 8 ? (unsigned)tile + 1u : 9u;
        asm volatile(ATTN_B_ASM
                     : [O] "+&v"(O), [L] "+&v"(Ls), [gate] "+&v"(gate), "+{v[128:143]}"(bs0), "+{v[160:175]}"(bneg), "+{v[176:191]}"(bcf)
                     : [q0] "v"(qb0), [q1] "v"(qb1), [ones] "v"(ones), [kv] "v"(kvb), [nt] "s"(nt)
                     : ATTN_B_CLOBBERS);
        PIN(gate);
        if (attn_bad(O, Ls)) {
            const int jd = head * 64 + tile, jf = (nt == 9) ? jd - 8 : -1;
            attn_safe(wsr, WS_KVB, jd - ((int)nt - 1), jd, jd, jf, qb0, qb1, ones, lane, O, Ls);
        }
        const float f = (1.0f - gate) / Ls[0];
#pragma unroll
        for (int e = 0; e < 16; ++e) O[e] *= f;
        st16(wsr, obase + 65536, pack8(O, 0)); st16(wsr, obase + 65536 + 1024, pack8(O, 1));
    }
    const int tb = (xcd >> 1) * 4 + (li & 3), nb = (xcd & 1) * 8 + (li >> 2), pw = w & 3;
    const unsigned f2 = WS_FLAG2 + (unsigned)((tq * 16 + hp) * 8 + w) * 4u;
    const srd_t wob = make_srd(woB, DM * DM * 2);
    unsigned wv_[2];
#pragma unroll
    for (int i = 0; i < 2; ++i) { const int row = pw * 16 + i * 8 + (lane >> 3); wv_[i] = (unsigned)(row * 2048 + nb * 128 + (((lane & 7) ^ (((lane >> 4) & 3) << 1)) << 4)); }
    const unsigned ob = WS_O + (unsigned)(tb * 4 + pw) * 131072u;
#define O_ISSUE(wsrd, wso, oso, sl) asm volatile( \
        "s_mov_b32 m0, %[mo]\n\ts_nop 0\n\tbuffer_load_dwordx4 %[lv], %[ws], %[os] offen sc1 lds\n\tbuffer_load_dwordx4 %[lv], %[ws], %[os] offen offset:1024 sc1 lds\n\t" \
        "buffer_load_dwordx4 %[lv], %[ws], %[os] offen offset:2048 sc1 lds\n\tbuffer_load_dwordx4 %[lv], %[ws], %[os] offen offset:3072 sc1 lds\n\t" \
        "s_mov_b32 m0, %[mw]\n\ts_nop 0\n\tbuffer_load_dwordx4 %[w0], %[wr], %[wo] offen lds\n\ts_add_u32 m0, %[mw], 0x400\n\ts_nop 0\n\tbuffer_load_dwordx4 %[w1], %[wr], %[wo] offen lds" \
        :: [mo] "s"(lds0 + (sl) + pw * 4096), [mw] "s"(lds0 + (sl) + 16384 + pw * 2048), [lv] "v"(lv), [ws] "s"(wsr), [os] "s"(ob + (oso)), \
           [w0] "v"(wv_[0]), [w1] "v"(wv_[1]), [wr] "s"(wsrd), [wo] "s"(wso) : "m0", "scc", "memory")
#define O_ISSUE_B(u, sl) O_ISSUE(wob, (unsigned)(u) * 131072u, (unsigned)((u) + 16) * 4096u, sl)
#define O_ISSUE_A(u, sl) O_ISSUE(woa, (unsigned)((u) - 16) * 131072u, (unsigned)((u) - 16) * 4096u, sl)
    {
        f32x16 OY = f32x16{}; f32x4 LY = f32x4{};
        O = f32x16{}; Ls = f32x4{};
        const unsigned vgrp = lv + (unsigned)grp * 16384u;
        unsigned cur = lds0 + vgrp, nxt = lds0 + A_SLOT + vgrp;
        unsigned sfar = lds0 + 2 * A_SLOT, cnt, tmp, early = 0;
        const unsigned k0a = lds0 + lv, vtag = tag;
#define ATTN_A_OUT [OX] "+&v"(O), [LX] "+&v"(Ls), [OY] "+&v"(OY), [LY] "+&v"(LY), [cur] "+&v"(cur), [nxt] "+&v"(nxt), [sfar] "+&s"(sfar), [cnt] "=&s"(cnt), [tmp] "=&s"(tmp)
#define ATTN_A_IN [qx0] "v"(qx0), [qx1] "v"(qx1), [qy0] "v"(qy0), [qy1] "v"(qy1), [ones] "v"(ones), [vlane] "v"(lv), [vgrp] "v"(vgrp), [k0a] "v"(k0a), [srd] "s"(wsr), [vtag] "v"(vtag)
        if (grp == 0) {
            unsigned soff = asoff + 2 * 16384u;
            const unsigned ldsw = (unsigned)w * 4096u;
            unsigned pm0 = lds0 + 4 * A_SLOT + (unsigned)w * 1024u, pm1 = lds0 + (unsigned)w * 1024u;
            const srd_t psrd = make_srd(li < 16 ? woA : woB, DM * DM * 2);
            unsigned psoff = (unsigned)(((li & 15) * 64 + w * 16) * 2048 + (xcd & 1) * 1024);
            const unsigned vfo = (w == 0 && lane == 0) ? WS_FLAGB + (unsigned)(tq * 16 + hp) * 4u : 0xFFFFFFF0u;
            asm volatile(ATTN_A0_ASM
                         : ATTN_A_OUT, [soff] "+&s"(soff), [psoff] "+&s"(psoff), [pm0] "+&s"(pm0), [pm1] "+&s"(pm1)
                         : ATTN_A_IN, [ldsw] "s"(ldsw), [psrd] "s"(psrd), [vfo] "v"(vfo)
                         : ATTN_CLOBBERS);
        } else {
            const unsigned vfb = WS_FLAGB + (unsigned)tb * 64u + (unsigned)(lane & 3) * 16u;
            const unsigned eos = ob + 16u * 4096u, emo = (unsigned)pw * 4096u;
            const unsigned vopf = ob + 65536u + (unsigned)((li >> 2) * 64 + lane) * 128u;
            asm volatile(ATTN_A1_ASM
                         : ATTN_A_OUT, [early] "+&s"(early)
                         : ATTN_A_IN, [vfb] "v"(vfb), [vopf] "v"(vopf), [eos] "s"(eos), [emo] "s"(emo), [wob] "s"(wob), [wv0] "v"(wv_[0]), [wv1] "v"(wv_[1])
                         : ATTN_CLOBBERS);
        }
#undef ATTN_A_OUT
#undef ATTN_A_IN
        const unsigned xb = (unsigned)(A_XCH + w * 4352), ib = (unsigned)(A_XCH + (w ^ 4) * 4352);
#pragma unroll
        for (int i = 0; i < 4; ++i) lds_st4(L + xb + i * 1024 + lv, __builtin_bit_cast(u32x4, (f32x4){OY[4 * i], OY[4 * i + 1], OY[4 * i + 2], OY[4 * i + 3]}));
        *(__attribute__((address_space(3))) float*)(L + xb + 4096 + lane * 4) = LY[0];
        WAITL_BAR();
        if (w >= 4) {
            if (!early) {
                poll_flags(wsr, WS_FLAGB + (unsigned)tb * 64u + (unsigned)(lane & 3) * 16u, tag);
                O_ISSUE_B(0, 0); O_ISSUE_B(1, O_STAGE);
            }
            O_ISSUE_B(2, 2 * O_STAGE);
        }
#pragma unroll
        for (int i = 0; i < 4; ++i) { const f32x4 t = __builtin_bit_cast(f32x4, lds_u4(L + ib + i * 1024 + lv)); O[4 * i] += t[0]; O[4 * i + 1] += t[1]; O[4 * i + 2] += t[2]; O[4 * i + 3] += t[3]; }
        Ls[0] += *(const __attribute__((address_space(3))) float*)(L + ib + 4096 + lane * 4);
        if (attn_bad(O, Ls)) attn_safe(wsr, WS_KVA, 0, 63, -1, -1, qx0, qx1, ones, lane, O, Ls);
        const float f = gate / Ls[0];
#pragma unroll
        for (int e = 0; e < 16; ++e) O[e] *= f;
        st16(wsr, obase, pack8(O, 0)); st16(wsr, obase + 1024, pack8(O, 1));
    }
    if (w >= 4) asm volatile("s_waitcnt vmcnt(14) lgkmcnt(0)\n\ts_barrier" ::: "memory");
    else asm volatile("s_waitcnt vmcnt(2) lgkmcnt(0)\n\ts_barrier" ::: "memory");
    if (w < 4) {
        const int wm = w >> 1, wn = w & 1;
        const int c = lane & 15, g = lane >> 4, q = c >> 2, pp = lane & 3;
        unsigned oa = lds0 + (unsigned)(2 * wm * 4096 + (g >> 1) * 1024 + (g & 1) * 512 + c * 16);
        unsigned wa = lds0 + (unsigned)(16384 + (16 * (g >> 1) + 4 * (g & 1) + q) * 128 + (((wn * 4 + (pp >> 1)) ^ ((2 * (g & 1) + (q >> 1)) << 1)) << 4) + 8 * (pp & 1));
        f32x16 acc0 = f32x16{}, acc1 = f32x16{};
        unsigned slot = 0, cnt, dl;
        PIN(acc0); PIN(acc1); PIN(oa); PIN(wa);
#define OUT_OUT "+{v[0:15]}"(acc0), "+{v[16:31]}"(acc1), [oa] "+&v"(oa), [wa] "+&v"(wa), [slot] "+&s"(slot), [cnt] "+&s"(cnt), [dl] "=&s"(dl)
        { const unsigned vtag = tag, vfo = lane == 0 ? f2 : 0xFFFFFFF0u;
          cnt = 10; asm volatile(OUT_ASM_HEAD : OUT_OUT : [vtag] "v"(vtag), [vfo] "v"(vfo), [srd] "s"(wsr) : OUT_CLOBBERS); }
        if (w == 0) poll_flags(wsr, WS_FLAG2 + (unsigned)tb * 512u + (unsigned)(lane & 31) * 16u, tag);
        cnt = 21; asm volatile(OUT_ASM_TAIL : OUT_OUT :: OUT_CLOBBERS);
#undef OUT_OUT
        const srd_t os = make_srd(out, SEQ * DM * 2);
#pragma unroll
        for (int i = 0; i < 4; ++i) {
            const f32x16& a = (i >> 1) ? acc1 : acc0; const int o = 8 * (i & 1);
            auto r0 = __builtin_amdgcn_permlane16_swap(pack2(a[o], a[o + 1]), pack2(a[o + 4], a[o + 5]), false, false);
            auto r1 = __builtin_amdgcn_permlane16_swap(pack2(a[o + 2], a[o + 3]), pack2(a[o + 6], a[o + 7]), false, false);
            const unsigned row = (unsigned)(tb * 128 + wm * 64 + i * 16 + c);
            __builtin_amdgcn_raw_buffer_store_b128((u32x4){r0[0], r1[0], r0[1], r1[1]}, os, row * 2048u + (unsigned)(nb * 64 + wn * 32 + (g & 1) * 16 + (g >> 1) * 8) * 2u, 0, 0);
        }
    } else {
        O_ISSUE_B(3, 3 * O_STAGE); O_ISSUE_B(4, 4 * O_STAGE); O_ISSUE_B(5, 5 * O_STAGE);
        WAITV(26); BAR();
        O_ISSUE_B(6, 0); WAITV(26); BAR();
        O_ISSUE_B(7, O_STAGE); WAITV(24); BAR();
        O_ISSUE_B(8, 2 * O_STAGE);
        __builtin_amdgcn_raw_buffer_store_b32(tag, wsr, lane == 0 ? f2 : 0xFFFFFFF0u, 0, SC1);
        WAITV(25); BAR();
        O_ISSUE_B(9, 3 * O_STAGE); WAITV(25); BAR();
        O_ISSUE_B(10, 4 * O_STAGE); WAITV(25); BAR();
        O_ISSUE_B(11, 5 * O_STAGE); WAITV(25); BAR();
        O_ISSUE_B(12, 0); WAITV(25); BAR();
        O_ISSUE_B(13, O_STAGE); WAITV(24); BAR();
        O_ISSUE_B(14, 2 * O_STAGE); WAITV(24); BAR();
        O_ISSUE_B(15, 3 * O_STAGE); WAITV(24); BAR();
        const srd_t woa = make_srd(woA, DM * DM * 2);
        unsigned sl = 4 * O_STAGE;
#pragma unroll 1
        for (int u = 16; u < 32; ++u) { O_ISSUE_A(u, sl); sl = (sl == 5 * O_STAGE) ? 0u : sl + O_STAGE; WAITV(24); BAR(); }
        WAITV(18); BAR(); WAITV(12); BAR(); WAITV(6); BAR(); WAITV(0); BAR();
#undef O_ISSUE
#undef O_ISSUE_A
#undef O_ISSUE_B
    }
}

extern "C" void kernel_launch(void* const* d_in, const int* in_sizes, int n_in, void* d_out, int out_size, void* d_ws, size_t ws_size,
                              hipStream_t stream) {
    k_fused<<<256, 512, 0, stream>>>((const __hip_bfloat16*)d_in[0], (const __hip_bfloat16*)d_in[1], (const __hip_bfloat16*)d_in[2], (const __hip_bfloat16*)d_in[3],
                                     (const __hip_bfloat16*)d_in[6], (const __hip_bfloat16*)d_in[8], (const __hip_bfloat16*)d_in[4], (const __hip_bfloat16*)d_in[7],
                                     (char*)d_ws, (__hip_bfloat16*)d_out);
}
```
